```python
import torch

def _softmax(x, axis=-1):
    return torch.softmax(x, dim=axis)

def _gelu(x):
    return torch.nn.functional.gelu(x, approximate='tanh')

def _silu(x):
    return torch.nn.functional.silu(x)

def _sigmoid(x):
    return torch.sigmoid(x)

def _layernorm(x, axis=-1, eps=1e-05):
    m = x.mean(dim=axis, keepdim=True)
    v = x.var(dim=axis, keepdim=True, unbiased=False)
    return (x - m) / torch.sqrt(v + eps)

def _scatter_add(tgt, idx, src, axis=0):
    return tgt.clone().index_add_(axis, idx, src)

def _segment_sum(src, seg, n):
    out = torch.zeros((n,) + src.shape[1:], dtype=src.dtype)
    return out.index_add_(0, seg, src)

def _pad(x, pw):
    flat = tuple((v for pair in reversed(pw) for v in pair))
    return torch.nn.functional.pad(x, flat)
kernel_name = 'parallel_sum__memory_aug_recurrent_mem_tr__glu_gated_geglu__prompt'

def setup_inputs():

    _w = lambda a: (lambda t: t.to(torch.bfloat16) if t.is_floating_point() else t)(torch.as_tensor(a))

    def _A_setup():
        B, S, Dm = (1, 2048, 512)
        H, Dh = (32, 128)
        M, seg = (8, 32)
        seg = min(seg, S)
        S = S // seg * seg
        return {'x': _w((lambda *s: torch.randn(*s, dtype=torch.bfloat16) * 0.05)(B, S, Dm)), 'mem0': _w((lambda *s: torch.randn(*s, dtype=torch.bfloat16) * 0.2)(B, M, Dm)), 'Wq': _w((lambda *s: torch.randn(*s, dtype=torch.bfloat16) * 1.6)(Dm, H, Dh) * Dm ** (-0.5)), 'Wk': _w((lambda *s: torch.randn(*s, dtype=torch.bfloat16) * 1.6)(Dm, H, Dh) * Dm ** (-0.5)), 'Wv': _w((lambda *s: torch.randn(*s, dtype=torch.bfloat16) * 0.2)(Dm, H, Dh) * Dm ** (-0.5)), 'Wo': _w((lambda *s: torch.randn(*s, dtype=torch.bfloat16) * 0.2)(H, Dh, Dm) * (H * Dh) ** (-0.5)), 'seg_len': seg}
    _A_in = _A_setup()

    def _B_setup():
        B, S, Dm, Df = (1, 2048, 512, 1792)
        return {'x': _w((lambda *s: torch.randn(*s, dtype=torch.bfloat16) * 0.05)(B, S, Dm)), 'w_gate': _w((lambda *s: torch.randn(*s, dtype=torch.bfloat16) * 0.4)(Dm, Df) * Dm ** (-0.5)), 'w_up': _w((lambda *s: torch.randn(*s, dtype=torch.bfloat16) * 0.4)(Dm, Df) * Dm ** (-0.5)), 'w_down': _w((lambda *s: torch.randn(*s, dtype=torch.bfloat16) * 0.4)(Df, Dm) * Df ** (-0.5))}
    _B_in = _B_setup()
    return {'A_x': _A_in['x'], 'A_mem0': _A_in['mem0'], 'A_Wq': _A_in['Wq'], 'A_Wk': _A_in['Wk'], 'A_Wv': _A_in['Wv'], 'A_Wo': _A_in['Wo'], 'A_seg_len': _A_in['seg_len'], 'B_x': _B_in['x'], 'B_w_gate': _B_in['w_gate'], 'B_w_up': _B_in['w_up'], 'B_w_down': _B_in['w_down']}

def reference(A_x, A_mem0, A_Wq, A_Wk, A_Wv, A_Wo, A_seg_len, B_x, B_w_gate, B_w_up, B_w_down):

    def _A_ref(x, mem0, Wq, Wk, Wv, Wo, seg_len):
        B, S, Dm = x.shape
        H, Dh = (Wq.shape[1], Wq.shape[2])
        M = mem0.shape[1]
        scale = Dh ** (-0.5)
        n_seg = S // seg_len
        L = M + seg_len
        idx = torch.arange(L)
        mask = (idx[:, None] < M) | (idx[:, None] >= idx[None, :])
        mem = mem0
        outs = []
        for s in range(n_seg):
            xs = x[:, s * seg_len:(s + 1) * seg_len, :].clone()
            h = torch.concatenate([mem, xs], axis=1)
            q = torch.einsum('btd,dhe->bhte', h, Wq)
            k = torch.einsum('btd,dhe->bhte', h, Wk)
            v = torch.einsum('btd,dhe->bhte', h, Wv)
            scores = torch.einsum('bhqe,bhke->bhqk', q, k) * scale
            scores = torch.where(mask[None, None], scores, -1000000000.0)
            p = _softmax(scores, axis=-1)
            o = torch.einsum('bhqk,bhke->bhqe', p, v)
            o = torch.einsum('bhte,hed->btd', o, Wo)
            mem = o[:, :M, :].clone()
            outs.append(o[:, M:, :])
        return torch.concatenate(outs, axis=1)

    def _B_ref(x, w_gate, w_up, w_down):
        g = torch.einsum('bsd,df->bsf', x, w_gate)
        u = torch.einsum('bsd,df->bsf', x, w_up)
        h = _gelu(g) * u
        return torch.einsum('bsf,fd->bsd', h, w_down)
    x = A_x
    xn = _layernorm(x, axis=-1)
    return x + _A_ref(xn, A_mem0, A_Wq, A_Wk, A_Wv, A_Wo, A_seg_len) + _B_ref(xn, B_w_gate, B_w_up, B_w_down)
```

```cpp
#include <hip/hip_runtime.h>
#include <hip/hip_bf16.h>
#include <stdint.h>

typedef __bf16 bf16x8 __attribute__((ext_vector_type(8)));
typedef __bf16 bf16x2 __attribute__((ext_vector_type(2)));
typedef float f32x4 __attribute__((ext_vector_type(4)));
typedef float f32x2 __attribute__((ext_vector_type(2)));
typedef unsigned u32x2 __attribute__((ext_vector_type(2)));
typedef unsigned u32x4 __attribute__((ext_vector_type(4)));
typedef unsigned short u16;

constexpr int DM = 512;
constexpr unsigned MB = 1u << 20;
constexpr unsigned OFF_XCH = 0, OFF_CTL = 2 * MB, OFF_XNF = 3 * MB, OFF_QF = 8 * MB, OFF_KV = 56 * MB, OFF_KM = 88 * MB, OFF_OF = 96 * MB, OFF_HF = 112 * MB, OFF_DBG = 140 * MB;
constexpr unsigned C_KV = 40960, C_O = 49152, C_H = 57344, C_PROG = 61440, C_END = 65536;
constexpr unsigned SENT = 0xFF81FF81u;

__device__ __forceinline__ float rbf(float f) { return (float)(__bf16)f; }
__device__ __forceinline__ unsigned pk2(float lo, float hi) { f32x2 v = {lo, hi}; return __builtin_bit_cast(unsigned, __builtin_convertvector(v, bf16x2)); }

constexpr unsigned C_VOTE = 0, C_EP = C_END, C_DONE = C_END + 256;
#define SC1 16
__device__ __forceinline__ void setup_share(const u16* x, const u16* mem0, char* ws, int b, unsigned tag) {
    const int lane = __builtin_amdgcn_mbcnt_hi(-1, __builtin_amdgcn_mbcnt_lo(-1, 0));
    const __amdgpu_buffer_rsrc_t rs = __builtin_amdgcn_make_buffer_rsrc(ws, 0, 0x0f000000, 0x00020000);
    const int row = b * 8 + (lane & 7);
    u32x4 v[8];
#pragma unroll
    for (int k = 0; k < 8; ++k) v[k] = *(const u32x4*)(x + (size_t)row * DM + ((lane >> 3) + 8 * k) * 8);
    const u32x4 sv = {SENT, SENT, SENT, SENT};
    const unsigned t = b * 64 + lane;
    u32x4 m = sv;
    if (t < 2048) {
        const unsigned o = (t * 16) & 8191, ks = o >> 10, i = ((o >> 8) & 3) * 2 + ((o >> 6) & 1), q0 = ((o >> 7) & 1) * 32 + ((o & 63) >> 1);
#pragma unroll
        for (int j = 0; j < 4; ++j) {
            const int qa = q0 + 2 * j, qb = qa + 1;
            m[j] = (unsigned)mem0[i * DM + 64 * ks + 16 * (qa & 3) + (qa >> 2)] | ((unsigned)mem0[i * DM + 64 * ks + 16 * (qb & 3) + (qb >> 2)] << 16);
            if (m[j] == SENT) m[j] = 0xffc1ffc1u;
        }
    }
#pragma unroll
    for (int k = 0; k < 6; ++k) __builtin_amdgcn_raw_buffer_store_b128(sv, rs, t * 16 + k * 262144, OFF_XCH, SC1);
    if (t >= 2048 && t < 4096) __builtin_amdgcn_raw_buffer_store_b128(sv, rs, (t - 2048) * 16, OFF_XCH + 112 * 16384, SC1);
    if (t >= 4096 && t < 8192) __builtin_amdgcn_raw_buffer_store_b128((u32x4){0u, 0u, 0u, 0u}, rs, (t - 4096) * 16, OFF_CTL, SC1);
    float s = 0;
#pragma unroll
    for (int k = 0; k < 8; ++k)
#pragma unroll
        for (int j = 0; j < 4; ++j) s += __uint_as_float(v[k][j] << 16) + __uint_as_float(v[k][j] & 0xffff0000u);
#pragma unroll
    for (int o = 32; o >= 8; o >>= 1) s += __shfl_xor(s, o);
    const float mean = s * (1.0f / DM);
    float q = 0;
#pragma unroll
    for (int k = 0; k < 8; ++k)
#pragma unroll
        for (int j = 0; j < 4; ++j) { const float d0 = __uint_as_float(v[k][j] << 16) - mean, d1 = __uint_as_float(v[k][j] & 0xffff0000u) - mean; q += d0 * d0 + d1 * d1; }
#pragma unroll
    for (int o = 32; o >= 8; o >>= 1) q += __shfl_xor(q, o);
    const float mb = rbf(mean), vb = rbf(q * (1.0f / DM));
    const float rsd = 1.0f / rbf(sqrtf(rbf(vb + 1e-5f)));
#pragma unroll
    for (int k = 0; k < 8; ++k) {
        u32x4 o;
#pragma unroll
        for (int j = 0; j < 4; ++j) o[j] = pk2(rbf(__uint_as_float(v[k][j] << 16) - mb) * rsd, rbf(__uint_as_float(v[k][j] & 0xffff0000u) - mb) * rsd);
        __builtin_amdgcn_raw_buffer_store_b128(o, rs, (row >> 4) * 16384 + (lane >> 3) * 256 + (row & 15) * 16 + k * 2048, OFF_XNF, SC1);
    }
    __builtin_amdgcn_raw_buffer_store_b128(m, rs, t * 16, OFF_XCH + 96 * 16384, SC1);
    asm volatile("s_waitcnt vmcnt(0)" ::: "memory");
    if (lane == 0) __builtin_amdgcn_raw_buffer_store_b32(tag, rs, b * 4, OFF_CTL + C_DONE, SC1);
}

struct Args { const u16* x; const u16* Wq; const u16* Wk; const u16* Wv; const u16* Wo; const u16* Wg; const u16* Wu; const u16* Wd; u16* out; char* ws; int d1; int d2; int forcenear; int mode; const u16* mem0; };
#define C8(p, n) #p #n "0", #p #n "1", #p #n "2", #p #n "3", #p #n "4", #p #n "5", #p #n "6", #p #n "7", #p #n "8", #p #n "9"
#define CALL(p) #p "0", #p "1", #p "2", #p "3", #p "4", #p "5", #p "6", #p "7", #p "8", #p "9", C8(p, 1), C8(p, 2), C8(p, 3), C8(p, 4), C8(p, 5), C8(p, 6), C8(p, 7), C8(p, 8), C8(p, 9), \
    C8(p, 10), C8(p, 11), C8(p, 12), C8(p, 13), C8(p, 14), C8(p, 15), C8(p, 16), C8(p, 17), C8(p, 18), C8(p, 19), C8(p, 20), C8(p, 21), C8(p, 22), C8(p, 23), C8(p, 24), \
    #p "250", #p "251", #p "252", #p "253", #p "254", #p "255"
#define BULK_CLOB CALL(v), CALL(a), C8(s, 4), C8(s, 5), C8(s, 6), C8(s, 7), "vcc", "scc", "memory"
__device__ __forceinline__ f32x4 mf(u32x4 a, u32x4 b, f32x4 c) { return __builtin_amdgcn_mfma_f32_16x16x32_bf16(__builtin_bit_cast(bf16x8, a), __builtin_bit_cast(bf16x8, b), c, 0, 0, 0); }
__device__ __forceinline__ float xmax(float v) {
    auto r = __builtin_amdgcn_permlane32_swap(__float_as_uint(v), __float_as_uint(v), false, false); v = fmaxf(__uint_as_float(r[0]), __uint_as_float(r[1]));
    auto q = __builtin_amdgcn_permlane16_swap(__float_as_uint(v), __float_as_uint(v), false, false); return fmaxf(__uint_as_float(q[0]), __uint_as_float(q[1]));
}
__device__ __forceinline__ unsigned sdist(unsigned m, unsigned a, unsigned b) { unsigned r = min(m, min(a ^ SENT, b ^ SENT)); asm("" : "+v"(r)); return r; }

#define TA(k)
__device__ __forceinline__ void attn_role(char* ws, int id) {
    const int lane = __builtin_amdgcn_mbcnt_hi(-1, __builtin_amdgcn_mbcnt_lo(-1, 0)), n = lane & 15, g = lane >> 4;
    const __amdgpu_buffer_rsrc_t rs = __builtin_amdgcn_make_buffer_rsrc(ws, 0, 0x0f000000, 0x00020000);
    const int h = id & 31;
    const f32x4 z4 = {0.f, 0.f, 0.f, 0.f};
    const u32x4 ones = {0x3f803f80u, 0x3f803f80u, 0x3f803f80u, 0x3f803f80u};
    const float cs = 0.08838834764831845f * 1.4426950408889634f;
    const unsigned kmvo = (2 * (n >> 2) + (n & 1)) * 256 + g * 16;
    bool dead = false;
    const unsigned long long tlim = __builtin_amdgcn_s_memrealtime() + 5000000ull;
    for (int s = id >> 5; s < 63; s += 8) {
        const unsigned sh = s * 32 + h;
        while (!dead) {
            unsigned f = __builtin_amdgcn_raw_buffer_load_b32(rs, 0, OFF_CTL + C_KV + h * 256 + s * 4, SC1);
            if (__builtin_amdgcn_readfirstlane(f) == 2) break;
            __builtin_amdgcn_s_sleep(32); asm volatile("" ::: "memory");
            if (__builtin_amdgcn_s_memrealtime() > tlim) dead = true;
        }
        asm volatile("" ::: "memory");
        u32x4 q[2][4], k[2][4], vf[8];
#pragma unroll
        for (int i = 0; i < 8; ++i) {
            q[i >> 2][i & 3] = __builtin_amdgcn_raw_buffer_load_b128(rs, lane * 16 + i * 1024, OFF_QF + sh * 8192, SC1);
            k[i >> 2][i & 3] = __builtin_amdgcn_raw_buffer_load_b128(rs, lane * 16 + i * 1024, OFF_KV + sh * 16384, SC1);
        }
#pragma unroll
        for (int i = 0; i < 8; ++i) vf[i] = __builtin_amdgcn_raw_buffer_load_b128(rs, lane * 16 + i * 1024, OFF_KV + sh * 16384 + 8192, SC1);
        f32x4 s00 = z4, s01 = z4, s11 = z4;
#pragma unroll
        for (int c = 0; c < 4; ++c) { s00 = mf(k[0][c], q[0][c], s00); s01 = mf(k[0][c], q[1][c], s01); s11 = mf(k[1][c], q[1][c], s11); }
#pragma unroll
        for (int r = 0; r < 4; ++r) { const bool ok = 4 * g + r <= n; s00[r] = ok ? s00[r] : -INFINITY; s11[r] = ok ? s11[r] : -INFINITY; }
        while (!dead) {
            const int f = __builtin_amdgcn_readfirstlane(__builtin_amdgcn_raw_buffer_load_b32(rs, 0, OFF_CTL + C_PROG + h * 128, SC1)) >> 1;
            if (f > s) break;
            const int lim = s >= 61 ? s - 1 : s;
            if (f >= lim) __builtin_amdgcn_s_sleep(3); else for (int k = f; k < lim; ++k) __builtin_amdgcn_s_sleep(36);
            asm volatile("" ::: "memory");
            if (__builtin_amdgcn_s_memrealtime() > tlim) dead = true;
        }
        TA(0);
        u32x4 km[4]; u32x2 vm[4];
        for (;;) {
            unsigned bad = ~0u;
#pragma unroll
            for (int c = 0; c < 4; ++c) { km[c] = __builtin_amdgcn_raw_buffer_load_b128(rs, kmvo + c * 64, OFF_KM + sh * 4096, SC1); vm[c] = __builtin_amdgcn_raw_buffer_load_b64(rs, 2048 + c * 512 + lane * 8, OFF_KM + sh * 4096, SC1); }
#pragma unroll
            for (int c = 0; c < 4; ++c) bad = sdist(sdist(sdist(bad, km[c][0], km[c][1]), km[c][2], km[c][3]), vm[c][0], vm[c][1]);
            if (!__any(bad == 0) || dead) break;
            __builtin_amdgcn_s_sleep(2); asm volatile("" ::: "memory");
            if (__builtin_amdgcn_s_memrealtime() > tlim) dead = true;
        }
        TA(1);
        f32x4 m0 = z4, m1 = z4;
#pragma unroll
        for (int c = 0; c < 4; ++c) { m0 = mf(km[c], q[0][c], m0); m1 = mf(km[c], q[1][c], m1); }
        const float x0 = xmax(fmaxf(fmaxf(fmaxf(s00[0], s00[1]), fmaxf(s00[2], s00[3])), fmaxf(m0[0], m0[1])));
        const float x1 = xmax(fmaxf(fmaxf(fmaxf(fmaxf(s01[0], s01[1]), fmaxf(s01[2], s01[3])), fmaxf(fmaxf(s11[0], s11[1]), fmaxf(s11[2], s11[3]))), fmaxf(m1[0], m1[1])));
        const float c0 = -x0 * cs, c1 = -x1 * cs;
#define EX(v, c) __builtin_amdgcn_exp2f(fmaf((v), cs, (c)))
        const u32x4 p0 = {pk2(EX(s00[0], c0), EX(s00[1], c0)), pk2(EX(s00[2], c0), EX(s00[3], c0)), 0u, 0u};
        const u32x4 p1 = {pk2(EX(s01[0], c1), EX(s01[1], c1)), pk2(EX(s01[2], c1), EX(s01[3], c1)), pk2(EX(s11[0], c1), EX(s11[1], c1)), pk2(EX(s11[2], c1), EX(s11[3], c1))};
        const u32x4 pm0 = {pk2(EX(m0[0], c0), EX(m0[1], c0)), 0u, 0u, 0u}, pm1 = {pk2(EX(m1[0], c1), EX(m1[1], c1)), 0u, 0u, 0u};
        const float r0 = __builtin_amdgcn_rcpf(mf(ones, pm0, mf(ones, p0, z4))[0]), r1 = __builtin_amdgcn_rcpf(mf(ones, pm1, mf(ones, p1, z4))[0]);
#pragma unroll
        for (int w = 0; w < 4; ++w) {
            const u32x4 a0 = {vm[w][0], 0u, 0u, 0u}, a1 = {vm[w][1], 0u, 0u, 0u};
            const f32x4 o00 = mf(a0, pm0, mf(vf[2 * w], p0, z4)), o01 = mf(a1, pm0, mf(vf[2 * w + 1], p0, z4));
            const f32x4 o10 = mf(a0, pm1, mf(vf[2 * w], p1, z4)), o11 = mf(a1, pm1, mf(vf[2 * w + 1], p1, z4));
            const u32x4 t0 = {pk2(o00[0] * r0, o00[1] * r0), pk2(o00[2] * r0, o00[3] * r0), pk2(o01[0] * r0, o01[1] * r0), pk2(o01[2] * r0, o01[3] * r0)};
            const u32x4 t1 = {pk2(o10[0] * r1, o10[1] * r1), pk2(o10[2] * r1, o10[3] * r1), pk2(o11[0] * r1, o11[1] * r1), pk2(o11[2] * r1, o11[3] * r1)};
            __builtin_amdgcn_raw_buffer_store_b128(t0, rs, lane * 16 + w * 1024, OFF_OF + (2 * s) * 131072 + h * 4096, SC1);
            __builtin_amdgcn_raw_buffer_store_b128(t1, rs, lane * 16 + w * 1024, OFF_OF + (2 * s + 1) * 131072 + h * 4096, SC1);
        }
        TA(2);
        asm volatile("s_waitcnt vmcnt(0)" ::: "memory");
        TA(3);
        if (lane == 0) __hip_atomic_fetch_add((unsigned*)(ws + OFF_CTL + C_O + s * 128), 1u, __ATOMIC_RELAXED, __HIP_MEMORY_SCOPE_AGENT);
    }
}

__global__ void __launch_bounds__(256, 1) k_main(Args a_) {
    extern __shared__ __attribute__((aligned(16))) char lds_[];
    int b = blockIdx.x;
    const int w = __builtin_amdgcn_readfirstlane(threadIdx.x >> 6);
    if ((b & 0x84) != 0) {
    asm volatile("" : "+s"(b));
    const int x = b & 7, i = b >> 3;
    const int h = (x & 3) * 8 + (i & 7), slot = i >> 3;
    const __attribute__((address_space(4))) Args* ap = (const __attribute__((address_space(4))) Args*)__builtin_amdgcn_kernarg_segment_ptr();
#define ARGS() asm volatile("" : "+s"(ap))
    ARGS();
    const int rank = x < 4 ? x * 16 + (i - 16) : (x - 4) * 32 + i;
    const int cg = rank >> 3, rg = rank & 7;
#define TS(k)
    TS(0);
    if (x >= 4) {
        asm volatile(
"  s_mov_b64 s[40:41], %0\n"
"  s_mov_b32 s42, 0x0f000000\n"
"  s_mov_b32 s43, 0x00020000\n"
"  s_mov_b32 s47, 0x00020000\n"
"  s_mov_b32 s50, %6\n"
"  v_mbcnt_lo_u32_b32 v1, -1, 0\n"
"  v_mbcnt_hi_u32_b32 v1, -1, v1\n"
"  v_lshlrev_b32 v0, 4, v1\n"
"  s_lshl_b32 s51, s50, 15\n"
"  s_add_u32 s51, s51, 32768\n"
"  s_getpc_b64 s[76:77]\n"
"L_cs%=:\n"
"  s_mov_b32 s78, L_ce%=-L_cs%=\n"
"  s_mov_b32 s79, 0x00020000\n"
"  v_lshlrev_b32 v124, 7, v1\n"
"  buffer_load_dword v125, v124, s[76:79], 0 offen\n"
"  v_add_u32 v124, 0x2000, v124\n"
"  buffer_load_dword v125, v124, s[76:79], 0 offen\n"
"  s_mov_b32 s52, %4\n"
"  s_mov_b32 s53, %5\n"
"  s_mov_b32 s62, 0xff81ff81\n"
"  s_lshl_b32 s75, s53, 5\n"
"  s_add_u32 s75, s75, s52\n"
"  s_lshl_b32 s49, s50, 13\n"
"  s_lshl_b32 s54, s53, 15\n"
"  s_add_u32 s54, s54, s49\n"
"  s_add_u32 s54, s54, 3145728\n"
"  s_mov_b32 s55, s49\n"
"  s_lshl_b32 s61, s52, 8\n"
"  s_mov_b32 s64, %7\n"
"  s_lshl_b32 s48, s50, 2\n"
"  s_add_u32 s64, s64, s48\n"
"  s_and_b32 s48, s52, 7\n"
"  s_lshl_b32 s48, s48, 4\n"
"  s_add_u32 s64, s64, s48\n"
"  s_lshl_b32 s64, s64, 13\n"
"  s_and_b32 s48, s61, 0x1800\n"
"  s_add_u32 s64, s64, s48\n"
"  s_mov_b32 s70, 4194304\n"
"  s_mov_b32 s71, 0x00020000\n"
"  s_mov_b64 s[68:69], %2\n"
"  s_add_u32 s48, s64, 0\n"
"  buffer_load_dwordx4 v[120:123], v0, s[68:71], s48 offen\n"
"  buffer_load_dwordx4 v[120:123], v0, s[68:71], s48 offen offset:1024\n"
"  s_add_u32 s48, s64, 8192\n"
"  buffer_load_dwordx4 v[120:123], v0, s[68:71], s48 offen\n"
"  buffer_load_dwordx4 v[120:123], v0, s[68:71], s48 offen offset:1024\n"
"  s_add_u32 s48, s64, 16384\n"
"  buffer_load_dwordx4 v[120:123], v0, s[68:71], s48 offen\n"
"  buffer_load_dwordx4 v[120:123], v0, s[68:71], s48 offen offset:1024\n"
"  s_add_u32 s48, s64, 24576\n"
"  buffer_load_dwordx4 v[120:123], v0, s[68:71], s48 offen\n"
"  buffer_load_dwordx4 v[120:123], v0, s[68:71], s48 offen offset:1024\n"
"  s_mov_b64 s[68:69], %3\n"
"  s_add_u32 s48, s64, 0\n"
"  buffer_load_dwordx4 v[120:123], v0, s[68:71], s48 offen\n"
"  buffer_load_dwordx4 v[120:123], v0, s[68:71], s48 offen offset:1024\n"
"  s_add_u32 s48, s64, 8192\n"
"  buffer_load_dwordx4 v[120:123], v0, s[68:71], s48 offen\n"
"  buffer_load_dwordx4 v[120:123], v0, s[68:71], s48 offen offset:1024\n"
"  s_add_u32 s48, s64, 16384\n"
"  buffer_load_dwordx4 v[120:123], v0, s[68:71], s48 offen\n"
"  buffer_load_dwordx4 v[120:123], v0, s[68:71], s48 offen offset:1024\n"
"  s_add_u32 s48, s64, 24576\n"
"  buffer_load_dwordx4 v[120:123], v0, s[68:71], s48 offen\n"
"  buffer_load_dwordx4 v[120:123], v0, s[68:71], s48 offen offset:1024\n"
"  s_add_u32 s72, s40, 2162688\n"
"  s_addc_u32 s73, s41, 0\n"
"  s_load_dword s73, s[72:73], 0\n"
"  v_lshrrev_b32 v2, 2, v1\n"
"  v_mul_u32_u24 v2, 8192, v2\n"
"  v_and_b32 v3, 0x30, v0\n"
"  v_add_u32 v2, v2, v3\n"
"  v_lshrrev_b32 v14, 4, v1\n"
"  v_mul_u32_u24 v14, 512, v14\n"
"  v_bfe_u32 v15, v1, 2, 2\n"
"  v_lshl_add_u32 v14, v15, 6, v14\n"
"  v_and_b32 v15, 3, v1\n"
"  v_lshl_add_u32 v14, v15, 3, v14\n"
"  v_add_u32 v14, s51, v14\n"
"  s_lshl_b32 s48, s50, 6\n"
"  s_add_u32 s61, s61, s48\n"
"  s_lshl_b32 s48, s53, 5\n"
"  s_add_u32 s48, s48, s52\n"
"  s_lshl_b32 s49, s50, 10\n"
"  s_lshl_b32 s56, s48, 14\n"
"  s_add_u32 s56, s56, 58720256\n"
"  s_add_u32 s56, s56, s49\n"
"  s_lshl_b32 s57, s48, 13\n"
"  s_add_u32 s57, s57, 8388608\n"
"  s_add_u32 s57, s57, s49\n"
"  s_lshl_b32 s58, s48, 12\n"
"  s_add_u32 s58, s58, 92274688\n"
"  s_add_u32 s58, s58, s49\n"
"  s_lshl_b32 s59, s52, 8\n"
"  s_lshl_b32 s48, s53, 2\n"
"  s_add_u32 s59, s59, s48\n"
"  s_add_u32 s59, s59, 2138112\n"
"  s_cmp_eq_u32 s50, 0\n"
"  s_cselect_b32 s59, s59, 0x7e000000\n"
"  s_mov_b32 s60, 0x7f000000\n"
"  s_mov_b32 s63, 0x7f000000\n"
"  v_mov_b32 v7, v0\n"
"  v_add_u32 v3, s49, v0\n"
"  v_mov_b32 v5, 1\n"
"  v_mov_b32 v6, 2\n"
"  v_mov_b32 v4, 0x7f000000\n"
"  v_cmp_eq_u32 vcc, 0, v1\n"
"  s_nop 1\n"
"  v_cndmask_b32 v4, v4, v1, vcc\n"
"  v_mov_b32 v8, s62\n"
"  v_mov_b32 v9, s62\n"
"  v_mov_b32 v10, s62\n"
"  v_mov_b32 v11, s62\n"
"  buffer_store_dwordx4 v[8:11], v0, s[40:43], s58 offen sc1\n"
"  s_waitcnt vmcnt(9)\n"
"  s_waitcnt lgkmcnt(0)\n"
"  s_add_u32 s73, s73, 1\n"
"  s_mov_b64 s[44:45], %2\n"
"  s_mov_b32 s46, 4194304\n"
"  v_add_u32 v113, 130048, v2\n"
"  v_add_u32 v114, 260096, v2\n"
"  v_add_u32 v115, 390144, v2\n"
"  s_add_u32 m0, s51, 0\n"
"  s_add_u32 s48, s61, 0\n"
"  buffer_load_dwordx4 v2, s[44:47], s48 offen lds\n"
"  buffer_load_dwordx4 v113, s[44:47], s48 offen offset:1024 lds\n"
"  buffer_load_dwordx4 v114, s[44:47], s48 offen offset:2048 lds\n"
"  buffer_load_dwordx4 v115, s[44:47], s48 offen offset:3072 lds\n"
"  s_add_u32 m0, s51, 4096\n"
"  s_add_u32 s48, s61, 524288\n"
"  buffer_load_dwordx4 v2, s[44:47], s48 offen lds\n"
"  buffer_load_dwordx4 v113, s[44:47], s48 offen offset:1024 lds\n"
"  buffer_load_dwordx4 v114, s[44:47], s48 offen offset:2048 lds\n"
"  buffer_load_dwordx4 v115, s[44:47], s48 offen offset:3072 lds\n"
"  s_add_u32 m0, s51, 8192\n"
"  s_add_u32 s48, s61, 1048576\n"
"  buffer_load_dwordx4 v2, s[44:47], s48 offen lds\n"
"  buffer_load_dwordx4 v113, s[44:47], s48 offen offset:1024 lds\n"
"  buffer_load_dwordx4 v114, s[44:47], s48 offen offset:2048 lds\n"
"  buffer_load_dwordx4 v115, s[44:47], s48 offen offset:3072 lds\n"
"  s_add_u32 m0, s51, 12288\n"
"  s_add_u32 s48, s61, 1572864\n"
"  buffer_load_dwordx4 v2, s[44:47], s48 offen lds\n"
"  buffer_load_dwordx4 v113, s[44:47], s48 offen offset:1024 lds\n"
"  buffer_load_dwordx4 v114, s[44:47], s48 offen offset:2048 lds\n"
"  buffer_load_dwordx4 v115, s[44:47], s48 offen offset:3072 lds\n"
"  s_add_u32 m0, s51, 16384\n"
"  s_add_u32 s48, s61, 2097152\n"
"  buffer_load_dwordx4 v2, s[44:47], s48 offen lds\n"
"  buffer_load_dwordx4 v113, s[44:47], s48 offen offset:1024 lds\n"
"  buffer_load_dwordx4 v114, s[44:47], s48 offen offset:2048 lds\n"
"  buffer_load_dwordx4 v115, s[44:47], s48 offen offset:3072 lds\n"
"  s_add_u32 m0, s51, 20480\n"
"  s_add_u32 s48, s61, 2621440\n"
"  buffer_load_dwordx4 v2, s[44:47], s48 offen lds\n"
"  buffer_load_dwordx4 v113, s[44:47], s48 offen offset:1024 lds\n"
"  buffer_load_dwordx4 v114, s[44:47], s48 offen offset:2048 lds\n"
"  buffer_load_dwordx4 v115, s[44:47], s48 offen offset:3072 lds\n"
"  s_add_u32 m0, s51, 24576\n"
"  s_add_u32 s48, s61, 3145728\n"
"  buffer_load_dwordx4 v2, s[44:47], s48 offen lds\n"
"  buffer_load_dwordx4 v113, s[44:47], s48 offen offset:1024 lds\n"
"  buffer_load_dwordx4 v114, s[44:47], s48 offen offset:2048 lds\n"
"  buffer_load_dwordx4 v115, s[44:47], s48 offen offset:3072 lds\n"
"  s_add_u32 m0, s51, 28672\n"
"  s_add_u32 s48, s61, 3670016\n"
"  buffer_load_dwordx4 v2, s[44:47], s48 offen lds\n"
"  buffer_load_dwordx4 v113, s[44:47], s48 offen offset:1024 lds\n"
"  buffer_load_dwordx4 v114, s[44:47], s48 offen offset:2048 lds\n"
"  buffer_load_dwordx4 v115, s[44:47], s48 offen offset:3072 lds\n"
"  s_waitcnt vmcnt(28)\n"
"  ds_read_b64_tr_b16 a[0:1], v14 offset:0\n"
"  ds_read_b64_tr_b16 a[2:3], v14 offset:256\n"
"  ds_read_b64_tr_b16 a[4:5], v14 offset:32\n"
"  ds_read_b64_tr_b16 a[6:7], v14 offset:288\n"
"  ds_read_b64_tr_b16 a[8:9], v14 offset:2048\n"
"  ds_read_b64_tr_b16 a[10:11], v14 offset:2304\n"
"  ds_read_b64_tr_b16 a[12:13], v14 offset:2080\n"
"  ds_read_b64_tr_b16 a[14:15], v14 offset:2336\n"
"  s_waitcnt vmcnt(24)\n"
"  ds_read_b64_tr_b16 a[16:17], v14 offset:4096\n"
"  ds_read_b64_tr_b16 a[18:19], v14 offset:4352\n"
"  ds_read_b64_tr_b16 a[20:21], v14 offset:4128\n"
"  ds_read_b64_tr_b16 a[22:23], v14 offset:4384\n"
"  s_waitcnt lgkmcnt(4)\n"
"  s_mov_b64 s[44:45], %3\n"
"  s_mov_b32 s46, 4194304\n"
"  s_add_u32 m0, s51, 0\n"
"  s_add_u32 s48, s61, 0\n"
"  buffer_load_dwordx4 v2, s[44:47], s48 offen lds\n"
"  buffer_load_dwordx4 v113, s[44:47], s48 offen offset:1024 lds\n"
"  buffer_load_dwordx4 v114, s[44:47], s48 offen offset:2048 lds\n"
"  buffer_load_dwordx4 v115, s[44:47], s48 offen offset:3072 lds\n"
"  ds_read_b64_tr_b16 a[24:25], v14 offset:6144\n"
"  ds_read_b64_tr_b16 a[26:27], v14 offset:6400\n"
"  ds_read_b64_tr_b16 a[28:29], v14 offset:6176\n"
"  ds_read_b64_tr_b16 a[30:31], v14 offset:6432\n"
"  s_waitcnt vmcnt(24)\n"
"  ds_read_b64_tr_b16 a[32:33], v14 offset:8192\n"
"  ds_read_b64_tr_b16 a[34:35], v14 offset:8448\n"
"  ds_read_b64_tr_b16 a[36:37], v14 offset:8224\n"
"  ds_read_b64_tr_b16 a[38:39], v14 offset:8480\n"
"  s_waitcnt lgkmcnt(4)\n"
"  s_add_u32 m0, s51, 4096\n"
"  s_add_u32 s48, s61, 524288\n"
"  buffer_load_dwordx4 v2, s[44:47], s48 offen lds\n"
"  buffer_load_dwordx4 v113, s[44:47], s48 offen offset:1024 lds\n"
"  buffer_load_dwordx4 v114, s[44:47], s48 offen offset:2048 lds\n"
"  buffer_load_dwordx4 v115, s[44:47], s48 offen offset:3072 lds\n"
"  ds_read_b64_tr_b16 a[40:41], v14 offset:10240\n"
"  ds_read_b64_tr_b16 a[42:43], v14 offset:10496\n"
"  ds_read_b64_tr_b16 a[44:45], v14 offset:10272\n"
"  ds_read_b64_tr_b16 a[46:47], v14 offset:10528\n"
"  s_waitcnt vmcnt(24)\n"
"  ds_read_b64_tr_b16 a[48:49], v14 offset:12288\n"
"  ds_read_b64_tr_b16 a[50:51], v14 offset:12544\n"
"  ds_read_b64_tr_b16 a[52:53], v14 offset:12320\n"
"  ds_read_b64_tr_b16 a[54:55], v14 offset:12576\n"
"  s_waitcnt lgkmcnt(4)\n"
"  s_add_u32 m0, s51, 8192\n"
"  s_add_u32 s48, s61, 1048576\n"
"  buffer_load_dwordx4 v2, s[44:47], s48 offen lds\n"
"  buffer_load_dwordx4 v113, s[44:47], s48 offen offset:1024 lds\n"
"  buffer_load_dwordx4 v114, s[44:47], s48 offen offset:2048 lds\n"
"  buffer_load_dwordx4 v115, s[44:47], s48 offen offset:3072 lds\n"
"  ds_read_b64_tr_b16 a[56:57], v14 offset:14336\n"
"  ds_read_b64_tr_b16 a[58:59], v14 offset:14592\n"
"  ds_read_b64_tr_b16 a[60:61], v14 offset:14368\n"
"  ds_read_b64_tr_b16 a[62:63], v14 offset:14624\n"
"  s_waitcnt vmcnt(24)\n"
"  ds_read_b64_tr_b16 a[64:65], v14 offset:16384\n"
"  ds_read_b64_tr_b16 a[66:67], v14 offset:16640\n"
"  ds_read_b64_tr_b16 a[68:69], v14 offset:16416\n"
"  ds_read_b64_tr_b16 a[70:71], v14 offset:16672\n"
"  s_waitcnt lgkmcnt(4)\n"
"  s_add_u32 m0, s51, 12288\n"
"  s_add_u32 s48, s61, 1572864\n"
"  buffer_load_dwordx4 v2, s[44:47], s48 offen lds\n"
"  buffer_load_dwordx4 v113, s[44:47], s48 offen offset:1024 lds\n"
"  buffer_load_dwordx4 v114, s[44:47], s48 offen offset:2048 lds\n"
"  buffer_load_dwordx4 v115, s[44:47], s48 offen offset:3072 lds\n"
"  ds_read_b64_tr_b16 a[72:73], v14 offset:18432\n"
"  ds_read_b64_tr_b16 a[74:75], v14 offset:18688\n"
"  ds_read_b64_tr_b16 a[76:77], v14 offset:18464\n"
"  ds_read_b64_tr_b16 a[78:79], v14 offset:18720\n"
"  s_waitcnt lgkmcnt(0)\n"
"  s_add_u32 m0, s51, 16384\n"
"  s_add_u32 s48, s61, 2097152\n"
"  buffer_load_dwordx4 v2, s[44:47], s48 offen lds\n"
"  buffer_load_dwordx4 v113, s[44:47], s48 offen offset:1024 lds\n"
"  buffer_load_dwordx4 v114, s[44:47], s48 offen offset:2048 lds\n"
"  buffer_load_dwordx4 v115, s[44:47], s48 offen offset:3072 lds\n"
"  s_mov_b32 s74, 2162944\n"
"  s_cmp_eq_u32 s50, 3\n"
"  s_cselect_b32 s74, s74, 0x7e000000\n"
"  v_not_b32 v104, s73\n"
"  v_not_b32 v105, s73\n"
"  v_not_b32 v106, s73\n"
"  v_not_b32 v107, s73\n"
"  buffer_load_dwordx4 v[104:107], v0, s[40:43], s74 offen sc1\n"
"  s_waitcnt vmcnt(28)\n"
"  ds_read_b64_tr_b16 a[80:81], v14 offset:20480\n"
"  ds_read_b64_tr_b16 a[82:83], v14 offset:20736\n"
"  ds_read_b64_tr_b16 a[84:85], v14 offset:20512\n"
"  ds_read_b64_tr_b16 a[86:87], v14 offset:20768\n"
"  ds_read_b64_tr_b16 a[88:89], v14 offset:22528\n"
"  ds_read_b64_tr_b16 a[90:91], v14 offset:22784\n"
"  ds_read_b64_tr_b16 a[92:93], v14 offset:22560\n"
"  ds_read_b64_tr_b16 a[94:95], v14 offset:22816\n"
"  s_waitcnt vmcnt(24)\n"
"  ds_read_b64_tr_b16 a[96:97], v14 offset:24576\n"
"  ds_read_b64_tr_b16 a[98:99], v14 offset:24832\n"
"  ds_read_b64_tr_b16 a[100:101], v14 offset:24608\n"
"  ds_read_b64_tr_b16 a[102:103], v14 offset:24864\n"
"  s_waitcnt lgkmcnt(4)\n"
"  s_add_u32 m0, s51, 20480\n"
"  s_add_u32 s48, s61, 2621440\n"
"  buffer_load_dwordx4 v2, s[44:47], s48 offen lds\n"
"  buffer_load_dwordx4 v113, s[44:47], s48 offen offset:1024 lds\n"
"  buffer_load_dwordx4 v114, s[44:47], s48 offen offset:2048 lds\n"
"  buffer_load_dwordx4 v115, s[44:47], s48 offen offset:3072 lds\n"
"  ds_read_b64_tr_b16 a[104:105], v14 offset:26624\n"
"  ds_read_b64_tr_b16 a[106:107], v14 offset:26880\n"
"  ds_read_b64_tr_b16 a[108:109], v14 offset:26656\n"
"  ds_read_b64_tr_b16 a[110:111], v14 offset:26912\n"
"  s_waitcnt lgkmcnt(0)\n"
"  s_add_u32 m0, s51, 24576\n"
"  s_add_u32 s48, s61, 3145728\n"
"  buffer_load_dwordx4 v2, s[44:47], s48 offen lds\n"
"  buffer_load_dwordx4 v113, s[44:47], s48 offen offset:1024 lds\n"
"  buffer_load_dwordx4 v114, s[44:47], s48 offen offset:2048 lds\n"
"  buffer_load_dwordx4 v115, s[44:47], s48 offen offset:3072 lds\n"
"  s_mov_b32 s72, 0\n"
"  s_cmp_lg_u32 s50, 3\n"
"  s_cbranch_scc1 L_dnd%=\n"
"L_dn%=:\n"
"  v_min3_u32 v108, v104, v105, v106\n"
"  v_max3_u32 v109, v104, v105, v106\n"
"  v_min_u32 v108, v108, v107\n"
"  v_max_u32 v109, v109, v107\n"
"  v_xor_b32 v108, s73, v108\n"
"  v_xor_b32 v109, s73, v109\n"
"  v_or_b32 v108, v108, v109\n"
"  v_cmp_ne_u32 vcc, 0, v108\n"
"  s_cbranch_vccz L_dnd%=\n"
"  s_add_u32 s72, s72, 1\n"
"  s_cmp_gt_u32 s72, 100000\n"
"  s_cbranch_scc1 L_dnd%=\n"
"  s_waitcnt vmcnt(0)\n"
"  s_sleep 2\n"
"  buffer_load_dwordx4 v[104:107], v0, s[40:43], s74 offen sc1\n"
"  s_waitcnt vmcnt(0)\n"
"  s_branch L_dn%=\n"
"L_dnd%=:\n"
"  s_barrier\n"
"  s_add_u32 s49, s54, 0\n"
"  buffer_load_dwordx4 v[16:19], v0, s[40:43], s49 offen offset:0 sc1\n"
"  buffer_load_dwordx4 v[20:23], v0, s[40:43], s49 offen offset:1024 sc1\n"
"  buffer_load_dwordx4 v[24:27], v0, s[40:43], s49 offen offset:2048 sc1\n"
"  buffer_load_dwordx4 v[28:31], v0, s[40:43], s49 offen offset:3072 sc1\n"
"  s_add_u32 s49, s54, 4096\n"
"  buffer_load_dwordx4 v[32:35], v0, s[40:43], s49 offen offset:0 sc1\n"
"  buffer_load_dwordx4 v[36:39], v0, s[40:43], s49 offen offset:1024 sc1\n"
"  buffer_load_dwordx4 v[40:43], v0, s[40:43], s49 offen offset:2048 sc1\n"
"  buffer_load_dwordx4 v[44:47], v0, s[40:43], s49 offen offset:3072 sc1\n"
"  s_waitcnt vmcnt(28)\n"
"  ds_read_b64_tr_b16 a[112:113], v14 offset:28672\n"
"  ds_read_b64_tr_b16 a[114:115], v14 offset:28928\n"
"  ds_read_b64_tr_b16 a[116:117], v14 offset:28704\n"
"  ds_read_b64_tr_b16 a[118:119], v14 offset:28960\n"
"  ds_read_b64_tr_b16 a[120:121], v14 offset:30720\n"
"  ds_read_b64_tr_b16 a[122:123], v14 offset:30976\n"
"  ds_read_b64_tr_b16 a[124:125], v14 offset:30752\n"
"  ds_read_b64_tr_b16 a[126:127], v14 offset:31008\n"
"  s_waitcnt lgkmcnt(0)\n"
"  s_add_u32 m0, s51, 28672\n"
"  s_add_u32 s48, s61, 3670016\n"
"  buffer_load_dwordx4 v2, s[44:47], s48 offen lds\n"
"  buffer_load_dwordx4 v113, s[44:47], s48 offen offset:1024 lds\n"
"  buffer_load_dwordx4 v114, s[44:47], s48 offen offset:2048 lds\n"
"  buffer_load_dwordx4 v115, s[44:47], s48 offen offset:3072 lds\n"
"  s_waitcnt vmcnt(0)\n"
"  v_add_u32 v108, s55, v0\n"
"  ds_write_b128 v108, v[16:19] offset:0\n"
"  ds_write_b128 v108, v[20:23] offset:1024\n"
"  ds_write_b128 v108, v[24:27] offset:2048\n"
"  ds_write_b128 v108, v[28:31] offset:3072\n"
"  ds_write_b128 v108, v[32:35] offset:4096\n"
"  ds_write_b128 v108, v[36:39] offset:5120\n"
"  ds_write_b128 v108, v[40:43] offset:6144\n"
"  ds_write_b128 v108, v[44:47] offset:7168\n"
"  s_waitcnt lgkmcnt(0)\n"
"  s_barrier\n"
"  ds_read_b128 v[64:67], v7 offset:0\n"
"  ds_read_b128 v[68:71], v7 offset:16384\n"
"  ds_read_b128 v[72:75], v7 offset:1024\n"
"  ds_read_b128 v[76:79], v7 offset:17408\n"
"  ds_read_b128 v[80:83], v7 offset:2048\n"
"  ds_read_b128 v[84:87], v7 offset:18432\n"
"  s_waitcnt lgkmcnt(5)\n"
"  v_mfma_f32_16x16x32_bf16 v[16:19], a[0:3], v[64:67], 0\n"
"  ds_read_b128 v[88:91], v7 offset:3072\n"
"  ds_read_b128 v[92:95], v7 offset:19456\n"
"  ds_read_b64_tr_b16 a[128:129], v14 offset:0\n"
"  v_mfma_f32_16x16x32_bf16 v[20:23], a[4:7], v[64:67], 0\n"
"  ds_read_b64_tr_b16 a[130:131], v14 offset:256\n"
"  s_waitcnt lgkmcnt(8)\n"
"  v_mfma_f32_16x16x32_bf16 v[24:27], a[0:3], v[68:71], 0\n"
"  ds_read_b64_tr_b16 a[132:133], v14 offset:32\n"
"  v_mfma_f32_16x16x32_bf16 v[28:31], a[4:7], v[68:71], 0\n"
"  ds_read_b64_tr_b16 a[134:135], v14 offset:288\n"
"  s_waitcnt lgkmcnt(9)\n"
"  v_mfma_f32_16x16x32_bf16 v[16:19], a[8:11], v[72:75], v[16:19]\n"
"  ds_read_b128 v[64:67], v7 offset:4096\n"
"  ds_read_b128 v[68:71], v7 offset:20480\n"
"  ds_read_b64_tr_b16 a[136:137], v14 offset:2048\n"
"  v_mfma_f32_16x16x32_bf16 v[20:23], a[12:15], v[72:75], v[20:23]\n"
"  ds_read_b64_tr_b16 a[138:139], v14 offset:2304\n"
"  s_waitcnt lgkmcnt(12)\n"
"  v_mfma_f32_16x16x32_bf16 v[24:27], a[8:11], v[76:79], v[24:27]\n"
"  ds_read_b64_tr_b16 a[140:141], v14 offset:2080\n"
"  v_mfma_f32_16x16x32_bf16 v[28:31], a[12:15], v[76:79], v[28:31]\n"
"  ds_read_b64_tr_b16 a[142:143], v14 offset:2336\n"
"  s_waitcnt lgkmcnt(13)\n"
"  v_mfma_f32_16x16x32_bf16 v[16:19], a[16:19], v[80:83], v[16:19]\n"
"  ds_read_b128 v[72:75], v7 offset:5120\n"
"  s_waitcnt lgkmcnt(10)\n"
"  ds_read_b128 v[76:79], v7 offset:21504\n"
"  ds_read_b64_tr_b16 a[144:145], v14 offset:4096\n"
"  v_mfma_f32_16x16x32_bf16 v[20:23], a[20:23], v[80:83], v[20:23]\n"
"  ds_read_b64_tr_b16 a[146:147], v14 offset:4352\n"
"  v_mfma_f32_16x16x32_bf16 v[24:27], a[16:19], v[84:87], v[24:27]\n"
"  ds_read_b64_tr_b16 a[148:149], v14 offset:4128\n"
"  v_mfma_f32_16x16x32_bf16 v[28:31], a[20:23], v[84:87], v[28:31]\n"
"  s_waitcnt lgkmcnt(10)\n"
"  ds_read_b64_tr_b16 a[150:151], v14 offset:4384\n"
"  v_mfma_f32_16x16x32_bf16 v[16:19], a[24:27], v[88:91], v[16:19]\n"
"  ds_read_b128 v[80:83], v7 offset:6144\n"
"  ds_read_b128 v[84:87], v7 offset:22528\n"
"  ds_read_b64_tr_b16 a[152:153], v14 offset:6144\n"
"  v_mfma_f32_16x16x32_bf16 v[20:23], a[28:31], v[88:91], v[20:23]\n"
"  s_waitcnt lgkmcnt(10)\n"
"  ds_read_b64_tr_b16 a[154:155], v14 offset:6400\n"
"  v_mfma_f32_16x16x32_bf16 v[24:27], a[24:27], v[92:95], v[24:27]\n"
"  ds_read_b64_tr_b16 a[156:157], v14 offset:6176\n"
"  v_mfma_f32_16x16x32_bf16 v[28:31], a[28:31], v[92:95], v[28:31]\n"
"  ds_read_b64_tr_b16 a[158:159], v14 offset:6432\n"
"  v_mfma_f32_16x16x32_bf16 v[16:19], a[32:35], v[64:67], v[16:19]\n"
"  ds_read_b128 v[88:91], v7 offset:7168\n"
"  s_waitcnt lgkmcnt(10)\n"
"  ds_read_b128 v[92:95], v7 offset:23552\n"
"  ds_read_b64_tr_b16 a[160:161], v14 offset:8192\n"
"  v_mfma_f32_16x16x32_bf16 v[20:23], a[36:39], v[64:67], v[20:23]\n"
"  ds_read_b64_tr_b16 a[162:163], v14 offset:8448\n"
"  v_mfma_f32_16x16x32_bf16 v[24:27], a[32:35], v[68:71], v[24:27]\n"
"  ds_read_b64_tr_b16 a[164:165], v14 offset:8224\n"
"  v_mfma_f32_16x16x32_bf16 v[28:31], a[36:39], v[68:71], v[28:31]\n"
"  s_waitcnt lgkmcnt(10)\n"
"  ds_read_b64_tr_b16 a[166:167], v14 offset:8480\n"
"  v_mfma_f32_16x16x32_bf16 v[16:19], a[40:43], v[72:75], v[16:19]\n"
"  ds_read_b128 v[64:67], v7 offset:8192\n"
"  ds_read_b128 v[68:71], v7 offset:24576\n"
"  ds_read_b64_tr_b16 a[168:169], v14 offset:10240\n"
"  v_mfma_f32_16x16x32_bf16 v[20:23], a[44:47], v[72:75], v[20:23]\n"
"  s_waitcnt lgkmcnt(10)\n"
"  ds_read_b64_tr_b16 a[170:171], v14 offset:10496\n"
"  v_mfma_f32_16x16x32_bf16 v[24:27], a[40:43], v[76:79], v[24:27]\n"
"  ds_read_b64_tr_b16 a[172:173], v14 offset:10272\n"
"  v_mfma_f32_16x16x32_bf16 v[28:31], a[44:47], v[76:79], v[28:31]\n"
"  ds_read_b64_tr_b16 a[174:175], v14 offset:10528\n"
"  v_mfma_f32_16x16x32_bf16 v[16:19], a[48:51], v[80:83], v[16:19]\n"
"  ds_read_b128 v[72:75], v7 offset:9216\n"
"  s_waitcnt lgkmcnt(10)\n"
"  ds_read_b128 v[76:79], v7 offset:25600\n"
"  ds_read_b64_tr_b16 a[176:177], v14 offset:12288\n"
"  v_mfma_f32_16x16x32_bf16 v[20:23], a[52:55], v[80:83], v[20:23]\n"
"  ds_read_b64_tr_b16 a[178:179], v14 offset:12544\n"
"  v_mfma_f32_16x16x32_bf16 v[24:27], a[48:51], v[84:87], v[24:27]\n"
"  ds_read_b64_tr_b16 a[180:181], v14 offset:12320\n"
"  v_mfma_f32_16x16x32_bf16 v[28:31], a[52:55], v[84:87], v[28:31]\n"
"  s_waitcnt lgkmcnt(10)\n"
"  ds_read_b64_tr_b16 a[182:183], v14 offset:12576\n"
"  v_mfma_f32_16x16x32_bf16 v[16:19], a[56:59], v[88:91], v[16:19]\n"
"  ds_read_b128 v[80:83], v7 offset:10240\n"
"  ds_read_b128 v[84:87], v7 offset:26624\n"
"  ds_read_b64_tr_b16 a[184:185], v14 offset:14336\n"
"  v_mfma_f32_16x16x32_bf16 v[20:23], a[60:63], v[88:91], v[20:23]\n"
"  s_waitcnt lgkmcnt(10)\n"
"  ds_read_b64_tr_b16 a[186:187], v14 offset:14592\n"
"  v_mfma_f32_16x16x32_bf16 v[24:27], a[56:59], v[92:95], v[24:27]\n"
"  ds_read_b64_tr_b16 a[188:189], v14 offset:14368\n"
"  v_mfma_f32_16x16x32_bf16 v[28:31], a[60:63], v[92:95], v[28:31]\n"
"  ds_read_b64_tr_b16 a[190:191], v14 offset:14624\n"
"  v_mfma_f32_16x16x32_bf16 v[16:19], a[64:67], v[64:67], v[16:19]\n"
"  ds_read_b128 v[88:91], v7 offset:11264\n"
"  s_waitcnt lgkmcnt(10)\n"
"  ds_read_b128 v[92:95], v7 offset:27648\n"
"  ds_read_b64_tr_b16 a[192:193], v14 offset:16384\n"
"  v_mfma_f32_16x16x32_bf16 v[20:23], a[68:71], v[64:67], v[20:23]\n"
"  ds_read_b64_tr_b16 a[194:195], v14 offset:16640\n"
"  v_mfma_f32_16x16x32_bf16 v[24:27], a[64:67], v[68:71], v[24:27]\n"
"  ds_read_b64_tr_b16 a[196:197], v14 offset:16416\n"
"  v_mfma_f32_16x16x32_bf16 v[28:31], a[68:71], v[68:71], v[28:31]\n"
"  s_waitcnt lgkmcnt(10)\n"
"  ds_read_b64_tr_b16 a[198:199], v14 offset:16672\n"
"  v_mfma_f32_16x16x32_bf16 v[16:19], a[72:75], v[72:75], v[16:19]\n"
"  ds_read_b128 v[64:67], v7 offset:12288\n"
"  ds_read_b128 v[68:71], v7 offset:28672\n"
"  ds_read_b64_tr_b16 a[200:201], v14 offset:18432\n"
"  v_mfma_f32_16x16x32_bf16 v[20:23], a[76:79], v[72:75], v[20:23]\n"
"  s_waitcnt lgkmcnt(10)\n"
"  ds_read_b64_tr_b16 a[202:203], v14 offset:18688\n"
"  v_mfma_f32_16x16x32_bf16 v[24:27], a[72:75], v[76:79], v[24:27]\n"
"  ds_read_b64_tr_b16 a[204:205], v14 offset:18464\n"
"  v_mfma_f32_16x16x32_bf16 v[28:31], a[76:79], v[76:79], v[28:31]\n"
"  ds_read_b64_tr_b16 a[206:207], v14 offset:18720\n"
"  v_mfma_f32_16x16x32_bf16 v[16:19], a[80:83], v[80:83], v[16:19]\n"
"  ds_read_b128 v[72:75], v7 offset:13312\n"
"  s_waitcnt lgkmcnt(10)\n"
"  ds_read_b128 v[76:79], v7 offset:29696\n"
"  ds_read_b64_tr_b16 a[208:209], v14 offset:20480\n"
"  v_mfma_f32_16x16x32_bf16 v[20:23], a[84:87], v[80:83], v[20:23]\n"
"  ds_read_b64_tr_b16 a[210:211], v14 offset:20736\n"
"  v_mfma_f32_16x16x32_bf16 v[24:27], a[80:83], v[84:87], v[24:27]\n"
"  ds_read_b64_tr_b16 a[212:213], v14 offset:20512\n"
"  v_mfma_f32_16x16x32_bf16 v[28:31], a[84:87], v[84:87], v[28:31]\n"
"  s_waitcnt lgkmcnt(10)\n"
"  ds_read_b64_tr_b16 a[214:215], v14 offset:20768\n"
"  v_mfma_f32_16x16x32_bf16 v[16:19], a[88:91], v[88:91], v[16:19]\n"
"  ds_read_b128 v[80:83], v7 offset:14336\n"
"  ds_read_b128 v[84:87], v7 offset:30720\n"
"  ds_read_b64_tr_b16 a[216:217], v14 offset:22528\n"
"  v_mfma_f32_16x16x32_bf16 v[20:23], a[92:95], v[88:91], v[20:23]\n"
"  s_waitcnt lgkmcnt(10)\n"
"  ds_read_b64_tr_b16 a[218:219], v14 offset:22784\n"
"  v_mfma_f32_16x16x32_bf16 v[24:27], a[88:91], v[92:95], v[24:27]\n"
"  ds_read_b64_tr_b16 a[220:221], v14 offset:22560\n"
"  v_mfma_f32_16x16x32_bf16 v[28:31], a[92:95], v[92:95], v[28:31]\n"
"  ds_read_b64_tr_b16 a[222:223], v14 offset:22816\n"
"  v_mfma_f32_16x16x32_bf16 v[16:19], a[96:99], v[64:67], v[16:19]\n"
"  ds_read_b128 v[88:91], v7 offset:15360\n"
"  s_waitcnt lgkmcnt(10)\n"
"  ds_read_b128 v[92:95], v7 offset:31744\n"
"  ds_read_b64_tr_b16 a[224:225], v14 offset:24576\n"
"  v_mfma_f32_16x16x32_bf16 v[20:23], a[100:103], v[64:67], v[20:23]\n"
"  ds_read_b64_tr_b16 a[226:227], v14 offset:24832\n"
"  v_mfma_f32_16x16x32_bf16 v[24:27], a[96:99], v[68:71], v[24:27]\n"
"  ds_read_b64_tr_b16 a[228:229], v14 offset:24608\n"
"  v_mfma_f32_16x16x32_bf16 v[28:31], a[100:103], v[68:71], v[28:31]\n"
"  s_waitcnt lgkmcnt(10)\n"
"  ds_read_b64_tr_b16 a[230:231], v14 offset:24864\n"
"  v_mfma_f32_16x16x32_bf16 v[16:19], a[104:107], v[72:75], v[16:19]\n"
"  ds_read_b128 v[64:67], v7 offset:0\n"
"  ds_read_b128 v[68:71], v7 offset:16384\n"
"  ds_read_b64_tr_b16 a[232:233], v14 offset:26624\n"
"  v_mfma_f32_16x16x32_bf16 v[20:23], a[108:111], v[72:75], v[20:23]\n"
"  s_waitcnt lgkmcnt(10)\n"
"  ds_read_b64_tr_b16 a[234:235], v14 offset:26880\n"
"  v_mfma_f32_16x16x32_bf16 v[24:27], a[104:107], v[76:79], v[24:27]\n"
"  ds_read_b64_tr_b16 a[236:237], v14 offset:26656\n"
"  v_mfma_f32_16x16x32_bf16 v[28:31], a[108:111], v[76:79], v[28:31]\n"
"  ds_read_b64_tr_b16 a[238:239], v14 offset:26912\n"
"  v_mfma_f32_16x16x32_bf16 v[16:19], a[112:115], v[80:83], v[16:19]\n"
"  ds_read_b128 v[72:75], v7 offset:1024\n"
"  s_waitcnt lgkmcnt(10)\n"
"  ds_read_b128 v[76:79], v7 offset:17408\n"
"  ds_read_b64_tr_b16 a[240:241], v14 offset:28672\n"
"  v_mfma_f32_16x16x32_bf16 v[20:23], a[116:119], v[80:83], v[20:23]\n"
"  ds_read_b64_tr_b16 a[242:243], v14 offset:28928\n"
"  v_mfma_f32_16x16x32_bf16 v[24:27], a[112:115], v[84:87], v[24:27]\n"
"  ds_read_b64_tr_b16 a[244:245], v14 offset:28704\n"
"  v_mfma_f32_16x16x32_bf16 v[28:31], a[116:119], v[84:87], v[28:31]\n"
"  s_waitcnt lgkmcnt(10)\n"
"  ds_read_b64_tr_b16 a[246:247], v14 offset:28960\n"
"  v_mfma_f32_16x16x32_bf16 v[16:19], a[120:123], v[88:91], v[16:19]\n"
"  ds_read_b128 v[80:83], v7 offset:2048\n"
"  ds_read_b128 v[84:87], v7 offset:18432\n"
"  ds_read_b64_tr_b16 a[248:249], v14 offset:30720\n"
"  v_mfma_f32_16x16x32_bf16 v[20:23], a[124:127], v[88:91], v[20:23]\n"
"  s_waitcnt lgkmcnt(10)\n"
"  ds_read_b64_tr_b16 a[250:251], v14 offset:30976\n"
"  v_mfma_f32_16x16x32_bf16 v[24:27], a[120:123], v[92:95], v[24:27]\n"
"  ds_read_b64_tr_b16 a[252:253], v14 offset:30752\n"
"  v_mfma_f32_16x16x32_bf16 v[28:31], a[124:127], v[92:95], v[28:31]\n"
"  ds_read_b64_tr_b16 a[254:255], v14 offset:31008\n"
"  v_mfma_f32_16x16x32_bf16 v[32:35], v[64:67], a[128:131], 0\n"
"  ds_read_b128 v[88:91], v7 offset:3072\n"
"  s_waitcnt lgkmcnt(10)\n"
"  ds_read_b128 v[92:95], v7 offset:19456\n"
"  v_mfma_f32_16x16x32_bf16 v[36:39], v[64:67], a[132:135], 0\n"
"  v_mfma_f32_16x16x32_bf16 v[40:43], v[68:71], a[128:131], 0\n"
"  v_mfma_f32_16x16x32_bf16 v[44:47], v[68:71], a[132:135], 0\n"
"  v_mfma_f32_16x16x32_bf16 v[32:35], v[72:75], a[136:139], v[32:35]\n"
"  ds_read_b128 v[64:67], v7 offset:4096\n"
"  ds_read_b128 v[68:71], v7 offset:20480\n"
"  v_mfma_f32_16x16x32_bf16 v[36:39], v[72:75], a[140:143], v[36:39]\n"
"  v_mfma_f32_16x16x32_bf16 v[40:43], v[76:79], a[136:139], v[40:43]\n"
"  v_mfma_f32_16x16x32_bf16 v[44:47], v[76:79], a[140:143], v[44:47]\n"
"  s_waitcnt lgkmcnt(9)\n"
"  v_mfma_f32_16x16x32_bf16 v[32:35], v[80:83], a[144:147], v[32:35]\n"
"  ds_read_b128 v[72:75], v7 offset:5120\n"
"  ds_read_b128 v[76:79], v7 offset:21504\n"
"  v_cvt_pk_bf16_f32 v96, v16, v20\n"
"  v_mfma_f32_16x16x32_bf16 v[36:39], v[80:83], a[148:151], v[36:39]\n"
"  v_cvt_pk_bf16_f32 v97, v17, v21\n"
"  s_waitcnt lgkmcnt(10)\n"
"  v_mfma_f32_16x16x32_bf16 v[40:43], v[84:87], a[144:147], v[40:43]\n"
"  v_cvt_pk_bf16_f32 v98, v18, v22\n"
"  v_mfma_f32_16x16x32_bf16 v[44:47], v[84:87], a[148:151], v[44:47]\n"
"  v_cvt_pk_bf16_f32 v99, v19, v23\n"
"  s_waitcnt lgkmcnt(5)\n"
"  v_mfma_f32_16x16x32_bf16 v[32:35], v[88:91], a[152:155], v[32:35]\n"
"  ds_read_b128 v[80:83], v7 offset:6144\n"
"  ds_read_b128 v[84:87], v7 offset:22528\n"
"  s_add_u32 s48, s56, 0\n"
"  buffer_store_dwordx4 v[96:99], v0, s[40:43], s48 offen sc1\n"
"  v_mfma_f32_16x16x32_bf16 v[36:39], v[88:91], a[156:159], v[36:39]\n"
"  v_cvt_pk_bf16_f32 v100, v24, v28\n"
"  s_waitcnt lgkmcnt(6)\n"
"  v_mfma_f32_16x16x32_bf16 v[40:43], v[92:95], a[152:155], v[40:43]\n"
"  v_cvt_pk_bf16_f32 v101, v25, v29\n"
"  v_mfma_f32_16x16x32_bf16 v[44:47], v[92:95], a[156:159], v[44:47]\n"
"  v_cvt_pk_bf16_f32 v102, v26, v30\n"
"  s_waitcnt lgkmcnt(5)\n"
"  v_mfma_f32_16x16x32_bf16 v[32:35], v[64:67], a[160:163], v[32:35]\n"
"  ds_read_b128 v[88:91], v7 offset:7168\n"
"  ds_read_b128 v[92:95], v7 offset:23552\n"
"  v_cvt_pk_bf16_f32 v103, v27, v31\n"
"  v_mfma_f32_16x16x32_bf16 v[36:39], v[64:67], a[164:167], v[36:39]\n"
"  s_add_u32 s48, s56, 4096\n"
"  buffer_store_dwordx4 v[100:103], v0, s[40:43], s48 offen sc1\n"
"  s_waitcnt lgkmcnt(6)\n"
"  v_mfma_f32_16x16x32_bf16 v[40:43], v[68:71], a[160:163], v[40:43]\n"
"  v_mfma_f32_16x16x32_bf16 v[44:47], v[68:71], a[164:167], v[44:47]\n"
"  s_waitcnt lgkmcnt(5)\n"
"  v_mfma_f32_16x16x32_bf16 v[32:35], v[72:75], a[168:171], v[32:35]\n"
"  ds_read_b128 v[64:67], v7 offset:8192\n"
"  ds_read_b128 v[68:71], v7 offset:24576\n"
"  v_mfma_f32_16x16x32_bf16 v[36:39], v[72:75], a[172:175], v[36:39]\n"
"  s_waitcnt lgkmcnt(6)\n"
"  v_mfma_f32_16x16x32_bf16 v[40:43], v[76:79], a[168:171], v[40:43]\n"
"  v_mfma_f32_16x16x32_bf16 v[44:47], v[76:79], a[172:175], v[44:47]\n"
"  s_waitcnt lgkmcnt(5)\n"
"  v_mfma_f32_16x16x32_bf16 v[32:35], v[80:83], a[176:179], v[32:35]\n"
"  ds_read_b128 v[72:75], v7 offset:9216\n"
"  ds_read_b128 v[76:79], v7 offset:25600\n"
"  v_mfma_f32_16x16x32_bf16 v[36:39], v[80:83], a[180:183], v[36:39]\n"
"  s_waitcnt lgkmcnt(6)\n"
"  v_mfma_f32_16x16x32_bf16 v[40:43], v[84:87], a[176:179], v[40:43]\n"
"  v_mfma_f32_16x16x32_bf16 v[44:47], v[84:87], a[180:183], v[44:47]\n"
"  s_waitcnt lgkmcnt(5)\n"
"  v_mfma_f32_16x16x32_bf16 v[32:35], v[88:91], a[184:187], v[32:35]\n"
"  ds_read_b128 v[80:83], v7 offset:10240\n"
"  ds_read_b128 v[84:87], v7 offset:26624\n"
"  v_mfma_f32_16x16x32_bf16 v[36:39], v[88:91], a[188:191], v[36:39]\n"
"  s_waitcnt lgkmcnt(6)\n"
"  v_mfma_f32_16x16x32_bf16 v[40:43], v[92:95], a[184:187], v[40:43]\n"
"  v_mfma_f32_16x16x32_bf16 v[44:47], v[92:95], a[188:191], v[44:47]\n"
"  s_waitcnt lgkmcnt(5)\n"
"  v_mfma_f32_16x16x32_bf16 v[32:35], v[64:67], a[192:195], v[32:35]\n"
"  ds_read_b128 v[88:91], v7 offset:11264\n"
"  ds_read_b128 v[92:95], v7 offset:27648\n"
"  v_mfma_f32_16x16x32_bf16 v[36:39], v[64:67], a[196:199], v[36:39]\n"
"  s_waitcnt lgkmcnt(6)\n"
"  v_mfma_f32_16x16x32_bf16 v[40:43], v[68:71], a[192:195], v[40:43]\n"
"  v_mfma_f32_16x16x32_bf16 v[44:47], v[68:71], a[196:199], v[44:47]\n"
"  s_waitcnt lgkmcnt(5)\n"
"  v_mfma_f32_16x16x32_bf16 v[32:35], v[72:75], a[200:203], v[32:35]\n"
"  ds_read_b128 v[64:67], v7 offset:12288\n"
"  ds_read_b128 v[68:71], v7 offset:28672\n"
"  v_mfma_f32_16x16x32_bf16 v[36:39], v[72:75], a[204:207], v[36:39]\n"
"  s_waitcnt lgkmcnt(6)\n"
"  v_mfma_f32_16x16x32_bf16 v[40:43], v[76:79], a[200:203], v[40:43]\n"
"  v_mfma_f32_16x16x32_bf16 v[44:47], v[76:79], a[204:207], v[44:47]\n"
"  s_waitcnt lgkmcnt(5)\n"
"  v_mfma_f32_16x16x32_bf16 v[32:35], v[80:83], a[208:211], v[32:35]\n"
"  ds_read_b128 v[72:75], v7 offset:13312\n"
"  ds_read_b128 v[76:79], v7 offset:29696\n"
"  v_mfma_f32_16x16x32_bf16 v[36:39], v[80:83], a[212:215], v[36:39]\n"
"  s_waitcnt lgkmcnt(6)\n"
"  v_mfma_f32_16x16x32_bf16 v[40:43], v[84:87], a[208:211], v[40:43]\n"
"  v_mfma_f32_16x16x32_bf16 v[44:47], v[84:87], a[212:215], v[44:47]\n"
"  s_waitcnt lgkmcnt(5)\n"
"  v_mfma_f32_16x16x32_bf16 v[32:35], v[88:91], a[216:219], v[32:35]\n"
"  ds_read_b128 v[80:83], v7 offset:14336\n"
"  ds_read_b128 v[84:87], v7 offset:30720\n"
"  v_mfma_f32_16x16x32_bf16 v[36:39], v[88:91], a[220:223], v[36:39]\n"
"  s_waitcnt lgkmcnt(6)\n"
"  v_mfma_f32_16x16x32_bf16 v[40:43], v[92:95], a[216:219], v[40:43]\n"
"  v_mfma_f32_16x16x32_bf16 v[44:47], v[92:95], a[220:223], v[44:47]\n"
"  s_waitcnt lgkmcnt(5)\n"
"  v_mfma_f32_16x16x32_bf16 v[32:35], v[64:67], a[224:227], v[32:35]\n"
"  ds_read_b128 v[88:91], v7 offset:15360\n"
"  ds_read_b128 v[92:95], v7 offset:31744\n"
"  v_mfma_f32_16x16x32_bf16 v[36:39], v[64:67], a[228:231], v[36:39]\n"
"  s_waitcnt lgkmcnt(6)\n"
"  v_mfma_f32_16x16x32_bf16 v[40:43], v[68:71], a[224:227], v[40:43]\n"
"  v_mfma_f32_16x16x32_bf16 v[44:47], v[68:71], a[228:231], v[44:47]\n"
"  s_waitcnt lgkmcnt(5)\n"
"  v_mfma_f32_16x16x32_bf16 v[32:35], v[72:75], a[232:235], v[32:35]\n"
"  v_mfma_f32_16x16x32_bf16 v[36:39], v[72:75], a[236:239], v[36:39]\n"
"  s_waitcnt lgkmcnt(4)\n"
"  v_mfma_f32_16x16x32_bf16 v[40:43], v[76:79], a[232:235], v[40:43]\n"
"  v_mfma_f32_16x16x32_bf16 v[44:47], v[76:79], a[236:239], v[44:47]\n"
"  s_waitcnt lgkmcnt(3)\n"
"  v_mfma_f32_16x16x32_bf16 v[32:35], v[80:83], a[240:243], v[32:35]\n"
"  v_mfma_f32_16x16x32_bf16 v[36:39], v[80:83], a[244:247], v[36:39]\n"
"  s_waitcnt lgkmcnt(2)\n"
"  v_mfma_f32_16x16x32_bf16 v[40:43], v[84:87], a[240:243], v[40:43]\n"
"  v_mfma_f32_16x16x32_bf16 v[44:47], v[84:87], a[244:247], v[44:47]\n"
"  s_waitcnt lgkmcnt(1)\n"
"  v_mfma_f32_16x16x32_bf16 v[32:35], v[88:91], a[248:251], v[32:35]\n"
"  v_mfma_f32_16x16x32_bf16 v[36:39], v[88:91], a[252:255], v[36:39]\n"
"  s_waitcnt lgkmcnt(0)\n"
"  v_mfma_f32_16x16x32_bf16 v[40:43], v[92:95], a[248:251], v[40:43]\n"
"  v_mfma_f32_16x16x32_bf16 v[44:47], v[92:95], a[252:255], v[44:47]\n"
"  s_nop 3\n"
"  v_cvt_pk_bf16_f32 v96, v32, v33\n"
"  v_cvt_pk_bf16_f32 v97, v34, v35\n"
"  s_nop 0\n"
"  v_cvt_pk_bf16_f32 v98, v40, v41\n"
"  v_cvt_pk_bf16_f32 v99, v42, v43\n"
"  s_add_u32 s48, s56, 8192\n"
"  buffer_store_dwordx4 v[96:99], v3, s[40:43], s48 offen offset:0 sc1\n"
"  v_cvt_pk_bf16_f32 v100, v36, v37\n"
"  v_cvt_pk_bf16_f32 v101, v38, v39\n"
"  v_cvt_pk_bf16_f32 v102, v44, v45\n"
"  v_cvt_pk_bf16_f32 v103, v46, v47\n"
"  s_add_u32 s48, s56, 8192\n"
"  buffer_store_dwordx4 v[100:103], v3, s[40:43], s48 offen offset:1024 sc1\n"
"  s_waitcnt vmcnt(0)\n"
"  s_barrier\n"
"  buffer_store_dword v5, v4, s[40:43], s59 offen sc1\n"
"  s_mov_b64 s[68:69], %1\n"
"  s_add_u32 s48, s64, 0\n"
"  buffer_load_dwordx4 v[120:123], v0, s[68:71], s48 offen\n"
"  buffer_load_dwordx4 v[120:123], v0, s[68:71], s48 offen offset:1024\n"
"  s_add_u32 s48, s64, 8192\n"
"  buffer_load_dwordx4 v[120:123], v0, s[68:71], s48 offen\n"
"  buffer_load_dwordx4 v[120:123], v0, s[68:71], s48 offen offset:1024\n"
"  s_add_u32 s48, s64, 16384\n"
"  buffer_load_dwordx4 v[120:123], v0, s[68:71], s48 offen\n"
"  buffer_load_dwordx4 v[120:123], v0, s[68:71], s48 offen offset:1024\n"
"  s_add_u32 s48, s64, 24576\n"
"  buffer_load_dwordx4 v[120:123], v0, s[68:71], s48 offen\n"
"  buffer_load_dwordx4 v[120:123], v0, s[68:71], s48 offen offset:1024\n"
"  s_waitcnt vmcnt(0)\n"
"  s_mov_b64 s[44:45], %1\n"
"  s_mov_b32 s46, 4194304\n"
"  s_add_u32 m0, s51, 0\n"
"  s_add_u32 s48, s61, 0\n"
"  buffer_load_dwordx4 v2, s[44:47], s48 offen lds\n"
"  buffer_load_dwordx4 v113, s[44:47], s48 offen offset:1024 lds\n"
"  buffer_load_dwordx4 v114, s[44:47], s48 offen offset:2048 lds\n"
"  buffer_load_dwordx4 v115, s[44:47], s48 offen offset:3072 lds\n"
"  s_add_u32 m0, s51, 4096\n"
"  s_add_u32 s48, s61, 524288\n"
"  buffer_load_dwordx4 v2, s[44:47], s48 offen lds\n"
"  buffer_load_dwordx4 v113, s[44:47], s48 offen offset:1024 lds\n"
"  buffer_load_dwordx4 v114, s[44:47], s48 offen offset:2048 lds\n"
"  buffer_load_dwordx4 v115, s[44:47], s48 offen offset:3072 lds\n"
"  s_add_u32 m0, s51, 8192\n"
"  s_add_u32 s48, s61, 1048576\n"
"  buffer_load_dwordx4 v2, s[44:47], s48 offen lds\n"
"  buffer_load_dwordx4 v113, s[44:47], s48 offen offset:1024 lds\n"
"  buffer_load_dwordx4 v114, s[44:47], s48 offen offset:2048 lds\n"
"  buffer_load_dwordx4 v115, s[44:47], s48 offen offset:3072 lds\n"
"  s_add_u32 m0, s51, 12288\n"
"  s_add_u32 s48, s61, 1572864\n"
"  buffer_load_dwordx4 v2, s[44:47], s48 offen lds\n"
"  buffer_load_dwordx4 v113, s[44:47], s48 offen offset:1024 lds\n"
"  buffer_load_dwordx4 v114, s[44:47], s48 offen offset:2048 lds\n"
"  buffer_load_dwordx4 v115, s[44:47], s48 offen offset:3072 lds\n"
"  s_add_u32 m0, s51, 16384\n"
"  s_add_u32 s48, s61, 2097152\n"
"  buffer_load_dwordx4 v2, s[44:47], s48 offen lds\n"
"  buffer_load_dwordx4 v113, s[44:47], s48 offen offset:1024 lds\n"
"  buffer_load_dwordx4 v114, s[44:47], s48 offen offset:2048 lds\n"
"  buffer_load_dwordx4 v115, s[44:47], s48 offen offset:3072 lds\n"
"  s_add_u32 m0, s51, 20480\n"
"  s_add_u32 s48, s61, 2621440\n"
"  buffer_load_dwordx4 v2, s[44:47], s48 offen lds\n"
"  buffer_load_dwordx4 v113, s[44:47], s48 offen offset:1024 lds\n"
"  buffer_load_dwordx4 v114, s[44:47], s48 offen offset:2048 lds\n"
"  buffer_load_dwordx4 v115, s[44:47], s48 offen offset:3072 lds\n"
"  s_add_u32 m0, s51, 24576\n"
"  s_add_u32 s48, s61, 3145728\n"
"  buffer_load_dwordx4 v2, s[44:47], s48 offen lds\n"
"  buffer_load_dwordx4 v113, s[44:47], s48 offen offset:1024 lds\n"
"  buffer_load_dwordx4 v114, s[44:47], s48 offen offset:2048 lds\n"
"  buffer_load_dwordx4 v115, s[44:47], s48 offen offset:3072 lds\n"
"  s_add_u32 m0, s51, 28672\n"
"  s_add_u32 s48, s61, 3670016\n"
"  buffer_load_dwordx4 v2, s[44:47], s48 offen lds\n"
"  buffer_load_dwordx4 v113, s[44:47], s48 offen offset:1024 lds\n"
"  buffer_load_dwordx4 v114, s[44:47], s48 offen offset:2048 lds\n"
"  buffer_load_dwordx4 v115, s[44:47], s48 offen offset:3072 lds\n"
"  s_cmp_lg_u32 s50, 3\n"
"  s_cbranch_scc1 L_ninv%=\n"
"  buffer_inv sc1\n"
"L_ninv%=:\n"
"  s_waitcnt vmcnt(28)\n"
"  ds_read_b64_tr_b16 v[128:129], v14 offset:0\n"
"  ds_read_b64_tr_b16 v[130:131], v14 offset:256\n"
"  ds_read_b64_tr_b16 v[132:133], v14 offset:32\n"
"  ds_read_b64_tr_b16 v[134:135], v14 offset:288\n"
"  ds_read_b64_tr_b16 v[136:137], v14 offset:2048\n"
"  ds_read_b64_tr_b16 v[138:139], v14 offset:2304\n"
"  ds_read_b64_tr_b16 v[140:141], v14 offset:2080\n"
"  ds_read_b64_tr_b16 v[142:143], v14 offset:2336\n"
"  s_waitcnt vmcnt(24)\n"
"  ds_read_b64_tr_b16 v[144:145], v14 offset:4096\n"
"  ds_read_b64_tr_b16 v[146:147], v14 offset:4352\n"
"  ds_read_b64_tr_b16 v[148:149], v14 offset:4128\n"
"  ds_read_b64_tr_b16 v[150:151], v14 offset:4384\n"
"  s_waitcnt lgkmcnt(4)\n"
"  ds_read_b64_tr_b16 v[152:153], v14 offset:6144\n"
"  ds_read_b64_tr_b16 v[154:155], v14 offset:6400\n"
"  ds_read_b64_tr_b16 v[156:157], v14 offset:6176\n"
"  ds_read_b64_tr_b16 v[158:159], v14 offset:6432\n"
"  s_waitcnt vmcnt(20)\n"
"  ds_read_b64_tr_b16 v[160:161], v14 offset:8192\n"
"  ds_read_b64_tr_b16 v[162:163], v14 offset:8448\n"
"  ds_read_b64_tr_b16 v[164:165], v14 offset:8224\n"
"  ds_read_b64_tr_b16 v[166:167], v14 offset:8480\n"
"  s_waitcnt lgkmcnt(4)\n"
"  ds_read_b64_tr_b16 v[168:169], v14 offset:10240\n"
"  ds_read_b64_tr_b16 v[170:171], v14 offset:10496\n"
"  ds_read_b64_tr_b16 v[172:173], v14 offset:10272\n"
"  ds_read_b64_tr_b16 v[174:175], v14 offset:10528\n"
"  s_waitcnt vmcnt(16)\n"
"  ds_read_b64_tr_b16 v[176:177], v14 offset:12288\n"
"  ds_read_b64_tr_b16 v[178:179], v14 offset:12544\n"
"  ds_read_b64_tr_b16 v[180:181], v14 offset:12320\n"
"  ds_read_b64_tr_b16 v[182:183], v14 offset:12576\n"
"  s_waitcnt lgkmcnt(4)\n"
"  ds_read_b64_tr_b16 v[184:185], v14 offset:14336\n"
"  ds_read_b64_tr_b16 v[186:187], v14 offset:14592\n"
"  ds_read_b64_tr_b16 v[188:189], v14 offset:14368\n"
"  ds_read_b64_tr_b16 v[190:191], v14 offset:14624\n"
"  s_waitcnt vmcnt(12)\n"
"  ds_read_b64_tr_b16 v[192:193], v14 offset:16384\n"
"  ds_read_b64_tr_b16 v[194:195], v14 offset:16640\n"
"  ds_read_b64_tr_b16 v[196:197], v14 offset:16416\n"
"  ds_read_b64_tr_b16 v[198:199], v14 offset:16672\n"
"  s_waitcnt lgkmcnt(4)\n"
"  ds_read_b64_tr_b16 v[200:201], v14 offset:18432\n"
"  ds_read_b64_tr_b16 v[202:203], v14 offset:18688\n"
"  ds_read_b64_tr_b16 v[204:205], v14 offset:18464\n"
"  ds_read_b64_tr_b16 v[206:207], v14 offset:18720\n"
"  s_waitcnt vmcnt(8)\n"
"  ds_read_b64_tr_b16 v[208:209], v14 offset:20480\n"
"  ds_read_b64_tr_b16 v[210:211], v14 offset:20736\n"
"  ds_read_b64_tr_b16 v[212:213], v14 offset:20512\n"
"  ds_read_b64_tr_b16 v[214:215], v14 offset:20768\n"
"  s_waitcnt lgkmcnt(4)\n"
"  ds_read_b64_tr_b16 v[216:217], v14 offset:22528\n"
"  ds_read_b64_tr_b16 v[218:219], v14 offset:22784\n"
"  ds_read_b64_tr_b16 v[220:221], v14 offset:22560\n"
"  ds_read_b64_tr_b16 v[222:223], v14 offset:22816\n"
"  s_waitcnt vmcnt(4)\n"
"  ds_read_b64_tr_b16 v[224:225], v14 offset:24576\n"
"  ds_read_b64_tr_b16 v[226:227], v14 offset:24832\n"
"  ds_read_b64_tr_b16 v[228:229], v14 offset:24608\n"
"  ds_read_b64_tr_b16 v[230:231], v14 offset:24864\n"
"  s_waitcnt lgkmcnt(4)\n"
"  ds_read_b64_tr_b16 v[232:233], v14 offset:26624\n"
"  ds_read_b64_tr_b16 v[234:235], v14 offset:26880\n"
"  ds_read_b64_tr_b16 v[236:237], v14 offset:26656\n"
"  ds_read_b64_tr_b16 v[238:239], v14 offset:26912\n"
"  s_waitcnt vmcnt(0)\n"
"  ds_read_b64_tr_b16 v[240:241], v14 offset:28672\n"
"  ds_read_b64_tr_b16 v[242:243], v14 offset:28928\n"
"  ds_read_b64_tr_b16 v[244:245], v14 offset:28704\n"
"  ds_read_b64_tr_b16 v[246:247], v14 offset:28960\n"
"  s_waitcnt lgkmcnt(4)\n"
"  ds_read_b64_tr_b16 v[248:249], v14 offset:30720\n"
"  ds_read_b64_tr_b16 v[250:251], v14 offset:30976\n"
"  ds_read_b64_tr_b16 v[252:253], v14 offset:30752\n"
"  ds_read_b64_tr_b16 v[254:255], v14 offset:31008\n"
"  s_waitcnt lgkmcnt(0)\n"
"  s_waitcnt vmcnt(0)\n"
"  s_barrier\n"
"  v_lshrrev_b32 v12, 4, v1\n"
"  v_mul_u32_u24 v12, 3584, v12\n"
"  v_and_b32 v13, 0xf0, v0\n"
"  v_add_u32 v12, v12, v13\n"
"  s_mov_b32 s70, 1835008\n"
"  s_mov_b32 s64, %10\n"
"  s_mul_i32 s48, s50, 14336\n"
"  s_add_u32 s64, s64, s48\n"
"  s_mov_b64 s[68:69], %8\n"
"  s_add_u32 s48, s64, 0\n"
"  buffer_load_dwordx4 v[120:123], v12, s[68:71], s48 offen\n"
"  s_add_u32 s48, s64, 57344\n"
"  buffer_load_dwordx4 v[120:123], v12, s[68:71], s48 offen\n"
"  s_add_u32 s48, s64, 114688\n"
"  buffer_load_dwordx4 v[120:123], v12, s[68:71], s48 offen\n"
"  s_add_u32 s48, s64, 172032\n"
"  buffer_load_dwordx4 v[120:123], v12, s[68:71], s48 offen\n"
"  s_mov_b64 s[68:69], %9\n"
"  s_add_u32 s48, s64, 0\n"
"  buffer_load_dwordx4 v[120:123], v12, s[68:71], s48 offen\n"
"  s_add_u32 s48, s64, 57344\n"
"  buffer_load_dwordx4 v[120:123], v12, s[68:71], s48 offen\n"
"  s_add_u32 s48, s64, 114688\n"
"  buffer_load_dwordx4 v[120:123], v12, s[68:71], s48 offen\n"
"  s_add_u32 s48, s64, 172032\n"
"  buffer_load_dwordx4 v[120:123], v12, s[68:71], s48 offen\n"
"  s_add_u32 s49, s53, 4\n"
"  s_cmp_lt_u32 s49, 64\n"
"  s_cselect_b32 s49, 131072, 0\n"
"  s_add_u32 s54, s54, s49\n"
"  s_xor_b32 s55, s55, 32768\n"
"  ds_read_b128 v[64:67], v7 offset:0\n"
"  ds_read_b128 v[68:71], v7 offset:16384\n"
"  ds_read_b128 v[72:75], v7 offset:1024\n"
"  ds_read_b128 v[76:79], v7 offset:17408\n"
"  ds_read_b128 v[80:83], v7 offset:2048\n"
"  ds_read_b128 v[84:87], v7 offset:18432\n"
"  s_waitcnt lgkmcnt(5)\n"
"  v_mfma_f32_16x16x32_bf16 v[48:51], v[128:131], v[64:67], 0\n"
"  ds_read_b128 v[88:91], v7 offset:3072\n"
"  ds_read_b128 v[92:95], v7 offset:19456\n"
"  v_mfma_f32_16x16x32_bf16 v[52:55], v[132:135], v[64:67], 0\n"
"  s_waitcnt lgkmcnt(6)\n"
"  v_mfma_f32_16x16x32_bf16 v[56:59], v[128:131], v[68:71], 0\n"
"  v_mfma_f32_16x16x32_bf16 v[60:63], v[132:135], v[68:71], 0\n"
"  s_waitcnt lgkmcnt(5)\n"
"  v_mfma_f32_16x16x32_bf16 v[48:51], v[136:139], v[72:75], v[48:51]\n"
"  ds_read_b128 v[64:67], v7 offset:4096\n"
"  ds_read_b128 v[68:71], v7 offset:20480\n"
"  v_mfma_f32_16x16x32_bf16 v[52:55], v[140:143], v[72:75], v[52:55]\n"
"  s_waitcnt lgkmcnt(6)\n"
"  v_mfma_f32_16x16x32_bf16 v[56:59], v[136:139], v[76:79], v[56:59]\n"
"  v_mfma_f32_16x16x32_bf16 v[60:63], v[140:143], v[76:79], v[60:63]\n"
"  s_waitcnt lgkmcnt(5)\n"
"  v_mfma_f32_16x16x32_bf16 v[48:51], v[144:147], v[80:83], v[48:51]\n"
"  ds_read_b128 v[72:75], v7 offset:5120\n"
"  ds_read_b128 v[76:79], v7 offset:21504\n"
"  s_add_u32 m0, s55, 0\n"
"  s_add_u32 s49, s54, 0\n"
"  buffer_load_dwordx4 v0, s[40:43], s49 offen lds\n"
"  v_mfma_f32_16x16x32_bf16 v[52:55], v[148:151], v[80:83], v[52:55]\n"
"  s_waitcnt lgkmcnt(6)\n"
"  v_mfma_f32_16x16x32_bf16 v[56:59], v[144:147], v[84:87], v[56:59]\n"
"  v_mfma_f32_16x16x32_bf16 v[60:63], v[148:151], v[84:87], v[60:63]\n"
"  s_waitcnt lgkmcnt(5)\n"
"  v_mfma_f32_16x16x32_bf16 v[48:51], v[152:155], v[88:91], v[48:51]\n"
"  ds_read_b128 v[80:83], v7 offset:6144\n"
"  ds_read_b128 v[84:87], v7 offset:22528\n"
"  s_add_u32 m0, s55, 1024\n"
"  s_add_u32 s49, s54, 1024\n"
"  buffer_load_dwordx4 v0, s[40:43], s49 offen lds\n"
"  v_mfma_f32_16x16x32_bf16 v[52:55], v[156:159], v[88:91], v[52:55]\n"
"  s_waitcnt lgkmcnt(6)\n"
"  v_mfma_f32_16x16x32_bf16 v[56:59], v[152:155], v[92:95], v[56:59]\n"
"  v_mfma_f32_16x16x32_bf16 v[60:63], v[156:159], v[92:95], v[60:63]\n"
"  s_waitcnt lgkmcnt(5)\n"
"  v_mfma_f32_16x16x32_bf16 v[48:51], v[160:163], v[64:67], v[48:51]\n"
"  ds_read_b128 v[88:91], v7 offset:7168\n"
"  ds_read_b128 v[92:95], v7 offset:23552\n"
"  s_add_u32 m0, s55, 2048\n"
"  s_add_u32 s49, s54, 2048\n"
"  buffer_load_dwordx4 v0, s[40:43], s49 offen lds\n"
"  v_mfma_f32_16x16x32_bf16 v[52:55], v[164:167], v[64:67], v[52:55]\n"
"  s_waitcnt lgkmcnt(6)\n"
"  v_mfma_f32_16x16x32_bf16 v[56:59], v[160:163], v[68:71], v[56:59]\n"
"  v_mfma_f32_16x16x32_bf16 v[60:63], v[164:167], v[68:71], v[60:63]\n"
"  s_waitcnt lgkmcnt(5)\n"
"  v_mfma_f32_16x16x32_bf16 v[48:51], v[168:171], v[72:75], v[48:51]\n"
"  ds_read_b128 v[64:67], v7 offset:8192\n"
"  ds_read_b128 v[68:71], v7 offset:24576\n"
"  s_add_u32 m0, s55, 3072\n"
"  s_add_u32 s49, s54, 3072\n"
"  buffer_load_dwordx4 v0, s[40:43], s49 offen lds\n"
"  v_mfma_f32_16x16x32_bf16 v[52:55], v[172:175], v[72:75], v[52:55]\n"
"  s_waitcnt lgkmcnt(6)\n"
"  v_mfma_f32_16x16x32_bf16 v[56:59], v[168:171], v[76:79], v[56:59]\n"
"  v_mfma_f32_16x16x32_bf16 v[60:63], v[172:175], v[76:79], v[60:63]\n"
"  s_waitcnt lgkmcnt(5)\n"
"  v_mfma_f32_16x16x32_bf16 v[48:51], v[176:179], v[80:83], v[48:51]\n"
"  ds_read_b128 v[72:75], v7 offset:9216\n"
"  ds_read_b128 v[76:79], v7 offset:25600\n"
"  s_add_u32 m0, s55, 4096\n"
"  s_add_u32 s49, s54, 4096\n"
"  buffer_load_dwordx4 v0, s[40:43], s49 offen lds\n"
"  v_mfma_f32_16x16x32_bf16 v[52:55], v[180:183], v[80:83], v[52:55]\n"
"  s_waitcnt lgkmcnt(6)\n"
"  v_mfma_f32_16x16x32_bf16 v[56:59], v[176:179], v[84:87], v[56:59]\n"
"  v_mfma_f32_16x16x32_bf16 v[60:63], v[180:183], v[84:87], v[60:63]\n"
"  s_waitcnt lgkmcnt(5)\n"
"  v_mfma_f32_16x16x32_bf16 v[48:51], v[184:187], v[88:91], v[48:51]\n"
"  ds_read_b128 v[80:83], v7 offset:10240\n"
"  ds_read_b128 v[84:87], v7 offset:26624\n"
"  s_add_u32 m0, s55, 5120\n"
"  s_add_u32 s49, s54, 5120\n"
"  buffer_load_dwordx4 v0, s[40:43], s49 offen lds\n"
"  v_mfma_f32_16x16x32_bf16 v[52:55], v[188:191], v[88:91], v[52:55]\n"
"  s_waitcnt lgkmcnt(6)\n"
"  v_mfma_f32_16x16x32_bf16 v[56:59], v[184:187], v[92:95], v[56:59]\n"
"  v_mfma_f32_16x16x32_bf16 v[60:63], v[188:191], v[92:95], v[60:63]\n"
"  s_waitcnt lgkmcnt(5)\n"
"  v_mfma_f32_16x16x32_bf16 v[48:51], v[192:195], v[64:67], v[48:51]\n"
"  ds_read_b128 v[88:91], v7 offset:11264\n"
"  ds_read_b128 v[92:95], v7 offset:27648\n"
"  s_add_u32 m0, s55, 6144\n"
"  s_add_u32 s49, s54, 6144\n"
"  buffer_load_dwordx4 v0, s[40:43], s49 offen lds\n"
"  v_mfma_f32_16x16x32_bf16 v[52:55], v[196:199], v[64:67], v[52:55]\n"
"  s_waitcnt lgkmcnt(6)\n"
"  v_mfma_f32_16x16x32_bf16 v[56:59], v[192:195], v[68:71], v[56:59]\n"
"  v_mfma_f32_16x16x32_bf16 v[60:63], v[196:199], v[68:71], v[60:63]\n"
"  s_waitcnt lgkmcnt(5)\n"
"  v_mfma_f32_16x16x32_bf16 v[48:51], v[200:203], v[72:75], v[48:51]\n"
"  ds_read_b128 v[64:67], v7 offset:12288\n"
"  ds_read_b128 v[68:71], v7 offset:28672\n"
"  s_add_u32 m0, s55, 7168\n"
"  s_add_u32 s49, s54, 7168\n"
"  buffer_load_dwordx4 v0, s[40:43], s49 offen lds\n"
"  v_mfma_f32_16x16x32_bf16 v[52:55], v[204:207], v[72:75], v[52:55]\n"
"  s_waitcnt lgkmcnt(6)\n"
"  v_mfma_f32_16x16x32_bf16 v[56:59], v[200:203], v[76:79], v[56:59]\n"
"  v_mfma_f32_16x16x32_bf16 v[60:63], v[204:207], v[76:79], v[60:63]\n"
"  s_waitcnt lgkmcnt(5)\n"
"  v_mfma_f32_16x16x32_bf16 v[48:51], v[208:211], v[80:83], v[48:51]\n"
"  ds_read_b128 v[72:75], v7 offset:13312\n"
"  ds_read_b128 v[76:79], v7 offset:29696\n"
"  v_mfma_f32_16x16x32_bf16 v[52:55], v[212:215], v[80:83], v[52:55]\n"
"  s_waitcnt lgkmcnt(6)\n"
"  v_mfma_f32_16x16x32_bf16 v[56:59], v[208:211], v[84:87], v[56:59]\n"
"  v_mfma_f32_16x16x32_bf16 v[60:63], v[212:215], v[84:87], v[60:63]\n"
"  s_waitcnt lgkmcnt(5)\n"
"  v_mfma_f32_16x16x32_bf16 v[48:51], v[216:219], v[88:91], v[48:51]\n"
"  ds_read_b128 v[80:83], v7 offset:14336\n"
"  ds_read_b128 v[84:87], v7 offset:30720\n"
"  v_mfma_f32_16x16x32_bf16 v[52:55], v[220:223], v[88:91], v[52:55]\n"
"  s_waitcnt lgkmcnt(6)\n"
"  v_mfma_f32_16x16x32_bf16 v[56:59], v[216:219], v[92:95], v[56:59]\n"
"  v_mfma_f32_16x16x32_bf16 v[60:63], v[220:223], v[92:95], v[60:63]\n"
"  s_waitcnt lgkmcnt(5)\n"
"  v_mfma_f32_16x16x32_bf16 v[48:51], v[224:227], v[64:67], v[48:51]\n"
"  ds_read_b128 v[88:91], v7 offset:15360\n"
"  ds_read_b128 v[92:95], v7 offset:31744\n"
"  v_mfma_f32_16x16x32_bf16 v[52:55], v[228:231], v[64:67], v[52:55]\n"
"  s_waitcnt lgkmcnt(6)\n"
"  v_mfma_f32_16x16x32_bf16 v[56:59], v[224:227], v[68:71], v[56:59]\n"
"  v_mfma_f32_16x16x32_bf16 v[60:63], v[228:231], v[68:71], v[60:63]\n"
"  s_waitcnt lgkmcnt(5)\n"
"  v_mfma_f32_16x16x32_bf16 v[48:51], v[232:235], v[72:75], v[48:51]\n"
"  v_mfma_f32_16x16x32_bf16 v[52:55], v[236:239], v[72:75], v[52:55]\n"
"  s_waitcnt lgkmcnt(4)\n"
"  v_mfma_f32_16x16x32_bf16 v[56:59], v[232:235], v[76:79], v[56:59]\n"
"  v_mfma_f32_16x16x32_bf16 v[60:63], v[236:239], v[76:79], v[60:63]\n"
"  s_waitcnt lgkmcnt(3)\n"
"  v_mfma_f32_16x16x32_bf16 v[48:51], v[240:243], v[80:83], v[48:51]\n"
"  v_mfma_f32_16x16x32_bf16 v[52:55], v[244:247], v[80:83], v[52:55]\n"
"  s_waitcnt lgkmcnt(2)\n"
"  v_mfma_f32_16x16x32_bf16 v[56:59], v[240:243], v[84:87], v[56:59]\n"
"  v_mfma_f32_16x16x32_bf16 v[60:63], v[244:247], v[84:87], v[60:63]\n"
"  s_waitcnt lgkmcnt(1)\n"
"  v_mfma_f32_16x16x32_bf16 v[48:51], v[248:251], v[88:91], v[48:51]\n"
"  v_mfma_f32_16x16x32_bf16 v[52:55], v[252:255], v[88:91], v[52:55]\n"
"  s_waitcnt lgkmcnt(0)\n"
"  v_mfma_f32_16x16x32_bf16 v[56:59], v[248:251], v[92:95], v[56:59]\n"
"  v_mfma_f32_16x16x32_bf16 v[60:63], v[252:255], v[92:95], v[60:63]\n"
"  s_waitcnt vmcnt(0)\n"
"  s_barrier\n"
"  s_branch L_p1e%=\n"
"L_p1%=:\n"
"  s_add_u32 s49, s53, 4\n"
"  s_cmp_lt_u32 s49, 64\n"
"  s_cselect_b32 s49, 131072, 0\n"
"  s_add_u32 s54, s54, s49\n"
"  s_xor_b32 s55, s55, 32768\n"
"  ds_read_b128 v[64:67], v7 offset:0\n"
"  ds_read_b128 v[68:71], v7 offset:16384\n"
"  ds_read_b128 v[72:75], v7 offset:1024\n"
"  ds_read_b128 v[76:79], v7 offset:17408\n"
"  ds_read_b128 v[80:83], v7 offset:2048\n"
"  ds_read_b128 v[84:87], v7 offset:18432\n"
"  s_waitcnt lgkmcnt(5)\n"
"  v_mfma_f32_16x16x32_bf16 v[16:19], a[0:3], v[64:67], 0\n"
"  ds_read_b128 v[88:91], v7 offset:3072\n"
"  ds_read_b128 v[92:95], v7 offset:19456\n"
"  v_mfma_f32_16x16x32_bf16 v[20:23], a[4:7], v[64:67], 0\n"
"  s_waitcnt lgkmcnt(6)\n"
"  v_mfma_f32_16x16x32_bf16 v[24:27], a[0:3], v[68:71], 0\n"
"  v_mfma_f32_16x16x32_bf16 v[28:31], a[4:7], v[68:71], 0\n"
"  s_waitcnt lgkmcnt(5)\n"
"  v_mfma_f32_16x16x32_bf16 v[16:19], a[8:11], v[72:75], v[16:19]\n"
"  ds_read_b128 v[64:67], v7 offset:4096\n"
"  ds_read_b128 v[68:71], v7 offset:20480\n"
"  v_mfma_f32_16x16x32_bf16 v[20:23], a[12:15], v[72:75], v[20:23]\n"
"  s_waitcnt lgkmcnt(6)\n"
"  v_mfma_f32_16x16x32_bf16 v[24:27], a[8:11], v[76:79], v[24:27]\n"
"  v_mfma_f32_16x16x32_bf16 v[28:31], a[12:15], v[76:79], v[28:31]\n"
"  s_waitcnt lgkmcnt(5)\n"
"  v_mfma_f32_16x16x32_bf16 v[16:19], a[16:19], v[80:83], v[16:19]\n"
"  ds_read_b128 v[72:75], v7 offset:5120\n"
"  ds_read_b128 v[76:79], v7 offset:21504\n"
"  v_cvt_pk_bf16_f32 v96, v48, v52\n"
"  v_mfma_f32_16x16x32_bf16 v[20:23], a[20:23], v[80:83], v[20:23]\n"
"  v_cvt_pk_bf16_f32 v97, v49, v53\n"
"  s_waitcnt lgkmcnt(6)\n"
"  v_mfma_f32_16x16x32_bf16 v[24:27], a[16:19], v[84:87], v[24:27]\n"
"  v_cvt_pk_bf16_f32 v98, v50, v54\n"
"  v_mfma_f32_16x16x32_bf16 v[28:31], a[20:23], v[84:87], v[28:31]\n"
"  v_cvt_pk_bf16_f32 v99, v51, v55\n"
"  s_waitcnt lgkmcnt(5)\n"
"  v_mfma_f32_16x16x32_bf16 v[16:19], a[24:27], v[88:91], v[16:19]\n"
"  ds_read_b128 v[80:83], v7 offset:6144\n"
"  ds_read_b128 v[84:87], v7 offset:22528\n"
"  s_add_u32 s48, s63, 0\n"
"  buffer_store_dwordx4 v[96:99], v0, s[40:43], s48 offen sc1\n"
"  v_mfma_f32_16x16x32_bf16 v[20:23], a[28:31], v[88:91], v[20:23]\n"
"  v_cvt_pk_bf16_f32 v100, v56, v60\n"
"  s_waitcnt lgkmcnt(6)\n"
"  v_mfma_f32_16x16x32_bf16 v[24:27], a[24:27], v[92:95], v[24:27]\n"
"  v_cvt_pk_bf16_f32 v101, v57, v61\n"
"  v_mfma_f32_16x16x32_bf16 v[28:31], a[28:31], v[92:95], v[28:31]\n"
"  v_cvt_pk_bf16_f32 v102, v58, v62\n"
"  s_waitcnt lgkmcnt(5)\n"
"  v_mfma_f32_16x16x32_bf16 v[16:19], a[32:35], v[64:67], v[16:19]\n"
"  ds_read_b128 v[88:91], v7 offset:7168\n"
"  ds_read_b128 v[92:95], v7 offset:23552\n"
"  v_cvt_pk_bf16_f32 v103, v59, v63\n"
"  v_mfma_f32_16x16x32_bf16 v[20:23], a[36:39], v[64:67], v[20:23]\n"
"  s_add_u32 s48, s63, 4096\n"
"  buffer_store_dwordx4 v[100:103], v0, s[40:43], s48 offen sc1\n"
"  s_waitcnt lgkmcnt(6)\n"
"  v_mfma_f32_16x16x32_bf16 v[24:27], a[32:35], v[68:71], v[24:27]\n"
"  s_add_u32 m0, s55, 0\n"
"  s_add_u32 s49, s54, 0\n"
"  buffer_load_dwordx4 v0, s[40:43], s49 offen lds\n"
"  v_mfma_f32_16x16x32_bf16 v[28:31], a[36:39], v[68:71], v[28:31]\n"
"  s_waitcnt lgkmcnt(5)\n"
"  v_mfma_f32_16x16x32_bf16 v[16:19], a[40:43], v[72:75], v[16:19]\n"
"  ds_read_b128 v[64:67], v7 offset:8192\n"
"  ds_read_b128 v[68:71], v7 offset:24576\n"
"  v_mfma_f32_16x16x32_bf16 v[20:23], a[44:47], v[72:75], v[20:23]\n"
"  s_waitcnt lgkmcnt(6)\n"
"  v_mfma_f32_16x16x32_bf16 v[24:27], a[40:43], v[76:79], v[24:27]\n"
"  s_add_u32 m0, s55, 1024\n"
"  s_add_u32 s49, s54, 1024\n"
"  buffer_load_dwordx4 v0, s[40:43], s49 offen lds\n"
"  v_mfma_f32_16x16x32_bf16 v[28:31], a[44:47], v[76:79], v[28:31]\n"
"  s_waitcnt lgkmcnt(5)\n"
"  v_mfma_f32_16x16x32_bf16 v[16:19], a[48:51], v[80:83], v[16:19]\n"
"  ds_read_b128 v[72:75], v7 offset:9216\n"
"  ds_read_b128 v[76:79], v7 offset:25600\n"
"  v_mfma_f32_16x16x32_bf16 v[20:23], a[52:55], v[80:83], v[20:23]\n"
"  s_waitcnt lgkmcnt(6)\n"
"  v_mfma_f32_16x16x32_bf16 v[24:27], a[48:51], v[84:87], v[24:27]\n"
"  s_add_u32 m0, s55, 2048\n"
"  s_add_u32 s49, s54, 2048\n"
"  buffer_load_dwordx4 v0, s[40:43], s49 offen lds\n"
"  v_mfma_f32_16x16x32_bf16 v[28:31], a[52:55], v[84:87], v[28:31]\n"
"  s_waitcnt lgkmcnt(5)\n"
"  v_mfma_f32_16x16x32_bf16 v[16:19], a[56:59], v[88:91], v[16:19]\n"
"  ds_read_b128 v[80:83], v7 offset:10240\n"
"  ds_read_b128 v[84:87], v7 offset:26624\n"
"  v_mfma_f32_16x16x32_bf16 v[20:23], a[60:63], v[88:91], v[20:23]\n"
"  s_waitcnt lgkmcnt(6)\n"
"  v_mfma_f32_16x16x32_bf16 v[24:27], a[56:59], v[92:95], v[24:27]\n"
"  s_add_u32 m0, s55, 3072\n"
"  s_add_u32 s49, s54, 3072\n"
"  buffer_load_dwordx4 v0, s[40:43], s49 offen lds\n"
"  v_mfma_f32_16x16x32_bf16 v[28:31], a[60:63], v[92:95], v[28:31]\n"
"  s_waitcnt lgkmcnt(5)\n"
"  v_mfma_f32_16x16x32_bf16 v[16:19], a[64:67], v[64:67], v[16:19]\n"
"  ds_read_b128 v[88:91], v7 offset:11264\n"
"  ds_read_b128 v[92:95], v7 offset:27648\n"
"  v_mfma_f32_16x16x32_bf16 v[20:23], a[68:71], v[64:67], v[20:23]\n"
"  s_waitcnt lgkmcnt(6)\n"
"  v_mfma_f32_16x16x32_bf16 v[24:27], a[64:67], v[68:71], v[24:27]\n"
"  s_add_u32 m0, s55, 4096\n"
"  s_add_u32 s49, s54, 4096\n"
"  buffer_load_dwordx4 v0, s[40:43], s49 offen lds\n"
"  v_mfma_f32_16x16x32_bf16 v[28:31], a[68:71], v[68:71], v[28:31]\n"
"  s_waitcnt lgkmcnt(5)\n"
"  v_mfma_f32_16x16x32_bf16 v[16:19], a[72:75], v[72:75], v[16:19]\n"
"  ds_read_b128 v[64:67], v7 offset:12288\n"
"  ds_read_b128 v[68:71], v7 offset:28672\n"
"  v_mfma_f32_16x16x32_bf16 v[20:23], a[76:79], v[72:75], v[20:23]\n"
"  s_waitcnt lgkmcnt(6)\n"
"  v_mfma_f32_16x16x32_bf16 v[24:27], a[72:75], v[76:79], v[24:27]\n"
"  s_add_u32 m0, s55, 5120\n"
"  s_add_u32 s49, s54, 5120\n"
"  buffer_load_dwordx4 v0, s[40:43], s49 offen lds\n"
"  v_mfma_f32_16x16x32_bf16 v[28:31], a[76:79], v[76:79], v[28:31]\n"
"  s_waitcnt lgkmcnt(5)\n"
"  v_mfma_f32_16x16x32_bf16 v[16:19], a[80:83], v[80:83], v[16:19]\n"
"  ds_read_b128 v[72:75], v7 offset:13312\n"
"  ds_read_b128 v[76:79], v7 offset:29696\n"
"  v_mfma_f32_16x16x32_bf16 v[20:23], a[84:87], v[80:83], v[20:23]\n"
"  s_waitcnt lgkmcnt(6)\n"
"  v_mfma_f32_16x16x32_bf16 v[24:27], a[80:83], v[84:87], v[24:27]\n"
"  s_add_u32 m0, s55, 6144\n"
"  s_add_u32 s49, s54, 6144\n"
"  buffer_load_dwordx4 v0, s[40:43], s49 offen lds\n"
"  v_mfma_f32_16x16x32_bf16 v[28:31], a[84:87], v[84:87], v[28:31]\n"
"  s_waitcnt lgkmcnt(5)\n"
"  v_mfma_f32_16x16x32_bf16 v[16:19], a[88:91], v[88:91], v[16:19]\n"
"  ds_read_b128 v[80:83], v7 offset:14336\n"
"  ds_read_b128 v[84:87], v7 offset:30720\n"
"  v_mfma_f32_16x16x32_bf16 v[20:23], a[92:95], v[88:91], v[20:23]\n"
"  s_waitcnt lgkmcnt(6)\n"
"  v_mfma_f32_16x16x32_bf16 v[24:27], a[88:91], v[92:95], v[24:27]\n"
"  s_add_u32 m0, s55, 7168\n"
"  s_add_u32 s49, s54, 7168\n"
"  buffer_load_dwordx4 v0, s[40:43], s49 offen lds\n"
"  v_mfma_f32_16x16x32_bf16 v[28:31], a[92:95], v[92:95], v[28:31]\n"
"  s_waitcnt lgkmcnt(5)\n"
"  v_mfma_f32_16x16x32_bf16 v[16:19], a[96:99], v[64:67], v[16:19]\n"
"  ds_read_b128 v[88:91], v7 offset:15360\n"
"  ds_read_b128 v[92:95], v7 offset:31744\n"
"  v_mfma_f32_16x16x32_bf16 v[20:23], a[100:103], v[64:67], v[20:23]\n"
"  s_waitcnt lgkmcnt(6)\n"
"  v_mfma_f32_16x16x32_bf16 v[24:27], a[96:99], v[68:71], v[24:27]\n"
"  v_mfma_f32_16x16x32_bf16 v[28:31], a[100:103], v[68:71], v[28:31]\n"
"  s_waitcnt lgkmcnt(5)\n"
"  v_mfma_f32_16x16x32_bf16 v[16:19], a[104:107], v[72:75], v[16:19]\n"
"  ds_read_b128 v[64:67], v7 offset:0\n"
"  ds_read_b128 v[68:71], v7 offset:16384\n"
"  v_mfma_f32_16x16x32_bf16 v[20:23], a[108:111], v[72:75], v[20:23]\n"
"  s_waitcnt lgkmcnt(6)\n"
"  v_mfma_f32_16x16x32_bf16 v[24:27], a[104:107], v[76:79], v[24:27]\n"
"  v_mfma_f32_16x16x32_bf16 v[28:31], a[108:111], v[76:79], v[28:31]\n"
"  s_waitcnt lgkmcnt(5)\n"
"  v_mfma_f32_16x16x32_bf16 v[16:19], a[112:115], v[80:83], v[16:19]\n"
"  ds_read_b128 v[72:75], v7 offset:1024\n"
"  ds_read_b128 v[76:79], v7 offset:17408\n"
"  v_mfma_f32_16x16x32_bf16 v[20:23], a[116:119], v[80:83], v[20:23]\n"
"  s_waitcnt lgkmcnt(6)\n"
"  v_mfma_f32_16x16x32_bf16 v[24:27], a[112:115], v[84:87], v[24:27]\n"
"  v_mfma_f32_16x16x32_bf16 v[28:31], a[116:119], v[84:87], v[28:31]\n"
"  s_waitcnt lgkmcnt(5)\n"
"  v_mfma_f32_16x16x32_bf16 v[16:19], a[120:123], v[88:91], v[16:19]\n"
"  ds_read_b128 v[80:83], v7 offset:2048\n"
"  ds_read_b128 v[84:87], v7 offset:18432\n"
"  v_mfma_f32_16x16x32_bf16 v[20:23], a[124:127], v[88:91], v[20:23]\n"
"  s_waitcnt lgkmcnt(6)\n"
"  v_mfma_f32_16x16x32_bf16 v[24:27], a[120:123], v[92:95], v[24:27]\n"
"  v_mfma_f32_16x16x32_bf16 v[28:31], a[124:127], v[92:95], v[28:31]\n"
"  s_waitcnt lgkmcnt(5)\n"
"  v_mfma_f32_16x16x32_bf16 v[32:35], v[64:67], a[128:131], 0\n"
"  ds_read_b128 v[88:91], v7 offset:3072\n"
"  ds_read_b128 v[92:95], v7 offset:19456\n"
"  v_mfma_f32_16x16x32_bf16 v[36:39], v[64:67], a[132:135], 0\n"
"  s_waitcnt lgkmcnt(6)\n"
"  v_mfma_f32_16x16x32_bf16 v[40:43], v[68:71], a[128:131], 0\n"
"  v_mfma_f32_16x16x32_bf16 v[44:47], v[68:71], a[132:135], 0\n"
"  s_waitcnt lgkmcnt(5)\n"
"  v_mfma_f32_16x16x32_bf16 v[32:35], v[72:75], a[136:139], v[32:35]\n"
"  ds_read_b128 v[64:67], v7 offset:4096\n"
"  ds_read_b128 v[68:71], v7 offset:20480\n"
"  v_mfma_f32_16x16x32_bf16 v[36:39], v[72:75], a[140:143], v[36:39]\n"
"  s_waitcnt lgkmcnt(6)\n"
"  v_mfma_f32_16x16x32_bf16 v[40:43], v[76:79], a[136:139], v[40:43]\n"
"  v_mfma_f32_16x16x32_bf16 v[44:47], v[76:79], a[140:143], v[44:47]\n"
"  s_waitcnt lgkmcnt(5)\n"
"  v_mfma_f32_16x16x32_bf16 v[32:35], v[80:83], a[144:147], v[32:35]\n"
"  ds_read_b128 v[72:75], v7 offset:5120\n"
"  ds_read_b128 v[76:79], v7 offset:21504\n"
"  v_cvt_pk_bf16_f32 v96, v16, v20\n"
"  v_mfma_f32_16x16x32_bf16 v[36:39], v[80:83], a[148:151], v[36:39]\n"
"  v_cvt_pk_bf16_f32 v97, v17, v21\n"
"  s_waitcnt lgkmcnt(6)\n"
"  v_mfma_f32_16x16x32_bf16 v[40:43], v[84:87], a[144:147], v[40:43]\n"
"  v_cvt_pk_bf16_f32 v98, v18, v22\n"
"  v_mfma_f32_16x16x32_bf16 v[44:47], v[84:87], a[148:151], v[44:47]\n"
"  v_cvt_pk_bf16_f32 v99, v19, v23\n"
"  s_waitcnt lgkmcnt(5)\n"
"  v_mfma_f32_16x16x32_bf16 v[32:35], v[88:91], a[152:155], v[32:35]\n"
"  ds_read_b128 v[80:83], v7 offset:6144\n"
"  ds_read_b128 v[84:87], v7 offset:22528\n"
"  s_add_u32 s48, s56, 0\n"
"  buffer_store_dwordx4 v[96:99], v0, s[40:43], s48 offen sc1\n"
"  v_mfma_f32_16x16x32_bf16 v[36:39], v[88:91], a[156:159], v[36:39]\n"
"  v_cvt_pk_bf16_f32 v100, v24, v28\n"
"  s_waitcnt lgkmcnt(6)\n"
"  v_mfma_f32_16x16x32_bf16 v[40:43], v[92:95], a[152:155], v[40:43]\n"
"  v_cvt_pk_bf16_f32 v101, v25, v29\n"
"  v_mfma_f32_16x16x32_bf16 v[44:47], v[92:95], a[156:159], v[44:47]\n"
"  v_cvt_pk_bf16_f32 v102, v26, v30\n"
"  s_waitcnt lgkmcnt(5)\n"
"  v_mfma_f32_16x16x32_bf16 v[32:35], v[64:67], a[160:163], v[32:35]\n"
"  ds_read_b128 v[88:91], v7 offset:7168\n"
"  ds_read_b128 v[92:95], v7 offset:23552\n"
"  v_cvt_pk_bf16_f32 v103, v27, v31\n"
"  v_mfma_f32_16x16x32_bf16 v[36:39], v[64:67], a[164:167], v[36:39]\n"
"  s_add_u32 s48, s56, 4096\n"
"  buffer_store_dwordx4 v[100:103], v0, s[40:43], s48 offen sc1\n"
"  s_waitcnt lgkmcnt(6)\n"
"  v_mfma_f32_16x16x32_bf16 v[40:43], v[68:71], a[160:163], v[40:43]\n"
"  buffer_store_dwordx4 v[8:11], v0, s[40:43], s58 offen sc1\n"
"  v_mfma_f32_16x16x32_bf16 v[44:47], v[68:71], a[164:167], v[44:47]\n"
"  s_waitcnt lgkmcnt(5)\n"
"  v_mfma_f32_16x16x32_bf16 v[32:35], v[72:75], a[168:171], v[32:35]\n"
"  ds_read_b128 v[64:67], v7 offset:8192\n"
"  ds_read_b128 v[68:71], v7 offset:24576\n"
"  v_mfma_f32_16x16x32_bf16 v[36:39], v[72:75], a[172:175], v[36:39]\n"
"  s_waitcnt lgkmcnt(6)\n"
"  v_mfma_f32_16x16x32_bf16 v[40:43], v[76:79], a[168:171], v[40:43]\n"
"  v_mfma_f32_16x16x32_bf16 v[44:47], v[76:79], a[172:175], v[44:47]\n"
"  s_waitcnt lgkmcnt(5)\n"
"  v_mfma_f32_16x16x32_bf16 v[32:35], v[80:83], a[176:179], v[32:35]\n"
"  ds_read_b128 v[72:75], v7 offset:9216\n"
"  ds_read_b128 v[76:79], v7 offset:25600\n"
"  v_mfma_f32_16x16x32_bf16 v[36:39], v[80:83], a[180:183], v[36:39]\n"
"  s_waitcnt lgkmcnt(6)\n"
"  v_mfma_f32_16x16x32_bf16 v[40:43], v[84:87], a[176:179], v[40:43]\n"
"  v_mfma_f32_16x16x32_bf16 v[44:47], v[84:87], a[180:183], v[44:47]\n"
"  s_waitcnt lgkmcnt(5)\n"
"  v_mfma_f32_16x16x32_bf16 v[32:35], v[88:91], a[184:187], v[32:35]\n"
"  ds_read_b128 v[80:83], v7 offset:10240\n"
"  ds_read_b128 v[84:87], v7 offset:26624\n"
"  v_mfma_f32_16x16x32_bf16 v[36:39], v[88:91], a[188:191], v[36:39]\n"
"  s_waitcnt lgkmcnt(6)\n"
"  v_mfma_f32_16x16x32_bf16 v[40:43], v[92:95], a[184:187], v[40:43]\n"
"  v_mfma_f32_16x16x32_bf16 v[44:47], v[92:95], a[188:191], v[44:47]\n"
"  s_waitcnt lgkmcnt(5)\n"
"  v_mfma_f32_16x16x32_bf16 v[32:35], v[64:67], a[192:195], v[32:35]\n"
"  ds_read_b128 v[88:91], v7 offset:11264\n"
"  ds_read_b128 v[92:95], v7 offset:27648\n"
"  v_mfma_f32_16x16x32_bf16 v[36:39], v[64:67], a[196:199], v[36:39]\n"
"  s_waitcnt lgkmcnt(6)\n"
"  v_mfma_f32_16x16x32_bf16 v[40:43], v[68:71], a[192:195], v[40:43]\n"
"  v_mfma_f32_16x16x32_bf16 v[44:47], v[68:71], a[196:199], v[44:47]\n"
"  s_waitcnt lgkmcnt(5)\n"
"  v_mfma_f32_16x16x32_bf16 v[32:35], v[72:75], a[200:203], v[32:35]\n"
"  ds_read_b128 v[64:67], v7 offset:12288\n"
"  ds_read_b128 v[68:71], v7 offset:28672\n"
"  v_mfma_f32_16x16x32_bf16 v[36:39], v[72:75], a[204:207], v[36:39]\n"
"  s_waitcnt lgkmcnt(6)\n"
"  v_mfma_f32_16x16x32_bf16 v[40:43], v[76:79], a[200:203], v[40:43]\n"
"  v_mfma_f32_16x16x32_bf16 v[44:47], v[76:79], a[204:207], v[44:47]\n"
"  s_waitcnt lgkmcnt(5)\n"
"  v_mfma_f32_16x16x32_bf16 v[32:35], v[80:83], a[208:211], v[32:35]\n"
"  ds_read_b128 v[72:75], v7 offset:13312\n"
"  ds_read_b128 v[76:79], v7 offset:29696\n"
"  v_mfma_f32_16x16x32_bf16 v[36:39], v[80:83], a[212:215], v[36:39]\n"
"  s_waitcnt lgkmcnt(6)\n"
"  v_mfma_f32_16x16x32_bf16 v[40:43], v[84:87], a[208:211], v[40:43]\n"
"  v_mfma_f32_16x16x32_bf16 v[44:47], v[84:87], a[212:215], v[44:47]\n"
"  s_waitcnt lgkmcnt(5)\n"
"  v_mfma_f32_16x16x32_bf16 v[32:35], v[88:91], a[216:219], v[32:35]\n"
"  ds_read_b128 v[80:83], v7 offset:14336\n"
"  ds_read_b128 v[84:87], v7 offset:30720\n"
"  v_mfma_f32_16x16x32_bf16 v[36:39], v[88:91], a[220:223], v[36:39]\n"
"  s_waitcnt lgkmcnt(6)\n"
"  v_mfma_f32_16x16x32_bf16 v[40:43], v[92:95], a[216:219], v[40:43]\n"
"  v_mfma_f32_16x16x32_bf16 v[44:47], v[92:95], a[220:223], v[44:47]\n"
"  s_waitcnt lgkmcnt(5)\n"
"  v_mfma_f32_16x16x32_bf16 v[32:35], v[64:67], a[224:227], v[32:35]\n"
"  ds_read_b128 v[88:91], v7 offset:15360\n"
"  ds_read_b128 v[92:95], v7 offset:31744\n"
"  v_mfma_f32_16x16x32_bf16 v[36:39], v[64:67], a[228:231], v[36:39]\n"
"  s_waitcnt lgkmcnt(6)\n"
"  v_mfma_f32_16x16x32_bf16 v[40:43], v[68:71], a[224:227], v[40:43]\n"
"  v_mfma_f32_16x16x32_bf16 v[44:47], v[68:71], a[228:231], v[44:47]\n"
"  s_waitcnt lgkmcnt(5)\n"
"  v_mfma_f32_16x16x32_bf16 v[32:35], v[72:75], a[232:235], v[32:35]\n"
"  ds_read_b128 v[64:67], v7 offset:0\n"
"  ds_read_b128 v[68:71], v7 offset:16384\n"
"  v_mfma_f32_16x16x32_bf16 v[36:39], v[72:75], a[236:239], v[36:39]\n"
"  s_waitcnt lgkmcnt(6)\n"
"  v_mfma_f32_16x16x32_bf16 v[40:43], v[76:79], a[232:235], v[40:43]\n"
"  v_mfma_f32_16x16x32_bf16 v[44:47], v[76:79], a[236:239], v[44:47]\n"
"  s_waitcnt lgkmcnt(5)\n"
"  v_mfma_f32_16x16x32_bf16 v[32:35], v[80:83], a[240:243], v[32:35]\n"
"  ds_read_b128 v[72:75], v7 offset:1024\n"
"  ds_read_b128 v[76:79], v7 offset:17408\n"
"  v_mfma_f32_16x16x32_bf16 v[36:39], v[80:83], a[244:247], v[36:39]\n"
"  s_waitcnt lgkmcnt(6)\n"
"  v_mfma_f32_16x16x32_bf16 v[40:43], v[84:87], a[240:243], v[40:43]\n"
"  v_mfma_f32_16x16x32_bf16 v[44:47], v[84:87], a[244:247], v[44:47]\n"
"  s_waitcnt lgkmcnt(5)\n"
"  v_mfma_f32_16x16x32_bf16 v[32:35], v[88:91], a[248:251], v[32:35]\n"
"  ds_read_b128 v[80:83], v7 offset:2048\n"
"  ds_read_b128 v[84:87], v7 offset:18432\n"
"  v_mfma_f32_16x16x32_bf16 v[36:39], v[88:91], a[252:255], v[36:39]\n"
"  s_waitcnt lgkmcnt(6)\n"
"  v_mfma_f32_16x16x32_bf16 v[40:43], v[92:95], a[248:251], v[40:43]\n"
"  v_mfma_f32_16x16x32_bf16 v[44:47], v[92:95], a[252:255], v[44:47]\n"
"  s_waitcnt lgkmcnt(5)\n"
"  v_mfma_f32_16x16x32_bf16 v[48:51], v[128:131], v[64:67], 0\n"
"  ds_read_b128 v[88:91], v7 offset:3072\n"
"  ds_read_b128 v[92:95], v7 offset:19456\n"
"  v_mfma_f32_16x16x32_bf16 v[52:55], v[132:135], v[64:67], 0\n"
"  s_waitcnt lgkmcnt(6)\n"
"  v_mfma_f32_16x16x32_bf16 v[56:59], v[128:131], v[68:71], 0\n"
"  v_mfma_f32_16x16x32_bf16 v[60:63], v[132:135], v[68:71], 0\n"
"  s_waitcnt lgkmcnt(5)\n"
"  v_mfma_f32_16x16x32_bf16 v[48:51], v[136:139], v[72:75], v[48:51]\n"
"  ds_read_b128 v[64:67], v7 offset:4096\n"
"  ds_read_b128 v[68:71], v7 offset:20480\n"
"  v_mfma_f32_16x16x32_bf16 v[52:55], v[140:143], v[72:75], v[52:55]\n"
"  s_waitcnt lgkmcnt(6)\n"
"  v_mfma_f32_16x16x32_bf16 v[56:59], v[136:139], v[76:79], v[56:59]\n"
"  v_mfma_f32_16x16x32_bf16 v[60:63], v[140:143], v[76:79], v[60:63]\n"
"  s_waitcnt lgkmcnt(5)\n"
"  v_mfma_f32_16x16x32_bf16 v[48:51], v[144:147], v[80:83], v[48:51]\n"
"  ds_read_b128 v[72:75], v7 offset:5120\n"
"  ds_read_b128 v[76:79], v7 offset:21504\n"
"  v_cvt_pk_bf16_f32 v96, v32, v33\n"
"  v_mfma_f32_16x16x32_bf16 v[52:55], v[148:151], v[80:83], v[52:55]\n"
"  v_cvt_pk_bf16_f32 v97, v34, v35\n"
"  s_waitcnt lgkmcnt(6)\n"
"  v_mfma_f32_16x16x32_bf16 v[56:59], v[144:147], v[84:87], v[56:59]\n"
"  v_cvt_pk_bf16_f32 v98, v40, v41\n"
"  v_mfma_f32_16x16x32_bf16 v[60:63], v[148:151], v[84:87], v[60:63]\n"
"  v_cvt_pk_bf16_f32 v99, v42, v43\n"
"  s_waitcnt lgkmcnt(5)\n"
"  v_mfma_f32_16x16x32_bf16 v[48:51], v[152:155], v[88:91], v[48:51]\n"
"  ds_read_b128 v[80:83], v7 offset:6144\n"
"  ds_read_b128 v[84:87], v7 offset:22528\n"
"  s_add_u32 s48, s56, 8192\n"
"  buffer_store_dwordx4 v[96:99], v3, s[40:43], s48 offen offset:0 sc1\n"
"  v_mfma_f32_16x16x32_bf16 v[52:55], v[156:159], v[88:91], v[52:55]\n"
"  v_cvt_pk_bf16_f32 v100, v36, v37\n"
"  s_waitcnt lgkmcnt(6)\n"
"  v_mfma_f32_16x16x32_bf16 v[56:59], v[152:155], v[92:95], v[56:59]\n"
"  v_cvt_pk_bf16_f32 v101, v38, v39\n"
"  v_mfma_f32_16x16x32_bf16 v[60:63], v[156:159], v[92:95], v[60:63]\n"
"  v_cvt_pk_bf16_f32 v102, v44, v45\n"
"  s_waitcnt lgkmcnt(5)\n"
"  v_mfma_f32_16x16x32_bf16 v[48:51], v[160:163], v[64:67], v[48:51]\n"
"  ds_read_b128 v[88:91], v7 offset:7168\n"
"  ds_read_b128 v[92:95], v7 offset:23552\n"
"  v_cvt_pk_bf16_f32 v103, v46, v47\n"
"  v_mfma_f32_16x16x32_bf16 v[52:55], v[164:167], v[64:67], v[52:55]\n"
"  s_add_u32 s48, s56, 8192\n"
"  buffer_store_dwordx4 v[100:103], v3, s[40:43], s48 offen offset:1024 sc1\n"
"  s_waitcnt lgkmcnt(6)\n"
"  v_mfma_f32_16x16x32_bf16 v[56:59], v[160:163], v[68:71], v[56:59]\n"
"  v_mfma_f32_16x16x32_bf16 v[60:63], v[164:167], v[68:71], v[60:63]\n"
"  s_waitcnt lgkmcnt(5)\n"
"  v_mfma_f32_16x16x32_bf16 v[48:51], v[168:171], v[72:75], v[48:51]\n"
"  ds_read_b128 v[64:67], v7 offset:8192\n"
"  ds_read_b128 v[68:71], v7 offset:24576\n"
"  v_mfma_f32_16x16x32_bf16 v[52:55], v[172:175], v[72:75], v[52:55]\n"
"  s_waitcnt lgkmcnt(6)\n"
"  v_mfma_f32_16x16x32_bf16 v[56:59], v[168:171], v[76:79], v[56:59]\n"
"  v_mfma_f32_16x16x32_bf16 v[60:63], v[172:175], v[76:79], v[60:63]\n"
"  s_waitcnt lgkmcnt(5)\n"
"  v_mfma_f32_16x16x32_bf16 v[48:51], v[176:179], v[80:83], v[48:51]\n"
"  ds_read_b128 v[72:75], v7 offset:9216\n"
"  ds_read_b128 v[76:79], v7 offset:25600\n"
"  v_mfma_f32_16x16x32_bf16 v[52:55], v[180:183], v[80:83], v[52:55]\n"
"  s_waitcnt lgkmcnt(6)\n"
"  v_mfma_f32_16x16x32_bf16 v[56:59], v[176:179], v[84:87], v[56:59]\n"
"  v_mfma_f32_16x16x32_bf16 v[60:63], v[180:183], v[84:87], v[60:63]\n"
"  s_waitcnt lgkmcnt(5)\n"
"  v_mfma_f32_16x16x32_bf16 v[48:51], v[184:187], v[88:91], v[48:51]\n"
"  ds_read_b128 v[80:83], v7 offset:10240\n"
"  ds_read_b128 v[84:87], v7 offset:26624\n"
"  v_mfma_f32_16x16x32_bf16 v[52:55], v[188:191], v[88:91], v[52:55]\n"
"  s_waitcnt lgkmcnt(6)\n"
"  v_mfma_f32_16x16x32_bf16 v[56:59], v[184:187], v[92:95], v[56:59]\n"
"  v_mfma_f32_16x16x32_bf16 v[60:63], v[188:191], v[92:95], v[60:63]\n"
"  s_waitcnt lgkmcnt(5)\n"
"  v_mfma_f32_16x16x32_bf16 v[48:51], v[192:195], v[64:67], v[48:51]\n"
"  ds_read_b128 v[88:91], v7 offset:11264\n"
"  ds_read_b128 v[92:95], v7 offset:27648\n"
"  v_mfma_f32_16x16x32_bf16 v[52:55], v[196:199], v[64:67], v[52:55]\n"
"  s_waitcnt lgkmcnt(6)\n"
"  v_mfma_f32_16x16x32_bf16 v[56:59], v[192:195], v[68:71], v[56:59]\n"
"  v_mfma_f32_16x16x32_bf16 v[60:63], v[196:199], v[68:71], v[60:63]\n"
"  s_waitcnt lgkmcnt(5)\n"
"  v_mfma_f32_16x16x32_bf16 v[48:51], v[200:203], v[72:75], v[48:51]\n"
"  ds_read_b128 v[64:67], v7 offset:12288\n"
"  ds_read_b128 v[68:71], v7 offset:28672\n"
"  v_mfma_f32_16x16x32_bf16 v[52:55], v[204:207], v[72:75], v[52:55]\n"
"  s_waitcnt lgkmcnt(6)\n"
"  v_mfma_f32_16x16x32_bf16 v[56:59], v[200:203], v[76:79], v[56:59]\n"
"  v_mfma_f32_16x16x32_bf16 v[60:63], v[204:207], v[76:79], v[60:63]\n"
"  s_waitcnt lgkmcnt(5)\n"
"  v_mfma_f32_16x16x32_bf16 v[48:51], v[208:211], v[80:83], v[48:51]\n"
"  ds_read_b128 v[72:75], v7 offset:13312\n"
"  ds_read_b128 v[76:79], v7 offset:29696\n"
"  v_mfma_f32_16x16x32_bf16 v[52:55], v[212:215], v[80:83], v[52:55]\n"
"  s_waitcnt lgkmcnt(6)\n"
"  v_mfma_f32_16x16x32_bf16 v[56:59], v[208:211], v[84:87], v[56:59]\n"
"  v_mfma_f32_16x16x32_bf16 v[60:63], v[212:215], v[84:87], v[60:63]\n"
"  s_waitcnt lgkmcnt(5)\n"
"  v_mfma_f32_16x16x32_bf16 v[48:51], v[216:219], v[88:91], v[48:51]\n"
"  ds_read_b128 v[80:83], v7 offset:14336\n"
"  ds_read_b128 v[84:87], v7 offset:30720\n"
"  v_mfma_f32_16x16x32_bf16 v[52:55], v[220:223], v[88:91], v[52:55]\n"
"  s_waitcnt lgkmcnt(6)\n"
"  v_mfma_f32_16x16x32_bf16 v[56:59], v[216:219], v[92:95], v[56:59]\n"
"  v_mfma_f32_16x16x32_bf16 v[60:63], v[220:223], v[92:95], v[60:63]\n"
"  s_waitcnt lgkmcnt(5)\n"
"  v_mfma_f32_16x16x32_bf16 v[48:51], v[224:227], v[64:67], v[48:51]\n"
"  ds_read_b128 v[88:91], v7 offset:15360\n"
"  ds_read_b128 v[92:95], v7 offset:31744\n"
"  v_mfma_f32_16x16x32_bf16 v[52:55], v[228:231], v[64:67], v[52:55]\n"
"  s_waitcnt lgkmcnt(6)\n"
"  v_mfma_f32_16x16x32_bf16 v[56:59], v[224:227], v[68:71], v[56:59]\n"
"  v_mfma_f32_16x16x32_bf16 v[60:63], v[228:231], v[68:71], v[60:63]\n"
"  s_waitcnt lgkmcnt(5)\n"
"  v_mfma_f32_16x16x32_bf16 v[48:51], v[232:235], v[72:75], v[48:51]\n"
"  v_mfma_f32_16x16x32_bf16 v[52:55], v[236:239], v[72:75], v[52:55]\n"
"  s_waitcnt lgkmcnt(4)\n"
"  v_mfma_f32_16x16x32_bf16 v[56:59], v[232:235], v[76:79], v[56:59]\n"
"  v_mfma_f32_16x16x32_bf16 v[60:63], v[236:239], v[76:79], v[60:63]\n"
"  s_waitcnt lgkmcnt(3)\n"
"  v_mfma_f32_16x16x32_bf16 v[48:51], v[240:243], v[80:83], v[48:51]\n"
"  v_mfma_f32_16x16x32_bf16 v[52:55], v[244:247], v[80:83], v[52:55]\n"
"  s_waitcnt lgkmcnt(2)\n"
"  v_mfma_f32_16x16x32_bf16 v[56:59], v[240:243], v[84:87], v[56:59]\n"
"  v_mfma_f32_16x16x32_bf16 v[60:63], v[244:247], v[84:87], v[60:63]\n"
"  s_waitcnt lgkmcnt(1)\n"
"  v_mfma_f32_16x16x32_bf16 v[48:51], v[248:251], v[88:91], v[48:51]\n"
"  v_mfma_f32_16x16x32_bf16 v[52:55], v[252:255], v[88:91], v[52:55]\n"
"  s_waitcnt lgkmcnt(0)\n"
"  v_mfma_f32_16x16x32_bf16 v[56:59], v[248:251], v[92:95], v[56:59]\n"
"  v_mfma_f32_16x16x32_bf16 v[60:63], v[252:255], v[92:95], v[60:63]\n"
"  s_waitcnt vmcnt(0)\n"
"  s_barrier\n"
"  buffer_store_dword v5, v4, s[40:43], s59 offen sc1\n"
"  buffer_store_dword v6, v4, s[40:43], s60 offen sc1\n"
"L_p1e%=:\n"
"  s_mov_b32 s60, s59\n"
"  s_mov_b32 s63, s57\n"
"  s_add_u32 s59, s59, 16\n"
"  s_add_u32 s56, s56, 2097152\n"
"  s_add_u32 s57, s57, 1048576\n"
"  s_add_u32 s58, s58, 524288\n"
"  v_xor_b32 v7, 0x8000, v7\n"
"  s_add_u32 s53, s53, 4\n"
"  s_cmp_lt_u32 s53, 64\n"
"  s_cbranch_scc1 L_p1%=\n"
"  v_cvt_pk_bf16_f32 v96, v48, v52\n"
"  v_cvt_pk_bf16_f32 v97, v49, v53\n"
"  v_cvt_pk_bf16_f32 v98, v50, v54\n"
"  v_cvt_pk_bf16_f32 v99, v51, v55\n"
"  s_add_u32 s48, s63, 0\n"
"  buffer_store_dwordx4 v[96:99], v0, s[40:43], s48 offen sc1\n"
"  v_cvt_pk_bf16_f32 v100, v56, v60\n"
"  v_cvt_pk_bf16_f32 v101, v57, v61\n"
"  v_cvt_pk_bf16_f32 v102, v58, v62\n"
"  v_cvt_pk_bf16_f32 v103, v59, v63\n"
"  s_add_u32 s48, s63, 4096\n"
"  buffer_store_dwordx4 v[100:103], v0, s[40:43], s48 offen sc1\n"
"  s_waitcnt vmcnt(0)\n"
"  s_barrier\n"
"  buffer_store_dword v6, v4, s[40:43], s60 offen sc1\n"
"L_ce%=:\n"
            :: "s"(ap->ws), "s"(ap->Wq), "s"(ap->Wk), "s"(ap->Wv), "s"(h), "s"(slot), "s"(w), "s"(slot * 128), "s"(ap->Wg), "s"(ap->Wu), "s"(cg < 14 ? rg * 64 * 3584 + cg * 256 : 0x7f000000)
            : BULK_CLOB);
        ARGS(); TS(1);
        if (cg < 14) {
            asm volatile(
"  s_mov_b64 s[40:41], %0\n"
"  s_mov_b32 s42, 0x0f000000\n"
"  s_mov_b32 s43, 0x00020000\n"
"  s_mov_b32 s47, 0x00020000\n"
"  s_mov_b32 s50, %5\n"
"  v_mbcnt_lo_u32_b32 v1, -1, 0\n"
"  v_mbcnt_hi_u32_b32 v1, -1, v1\n"
"  v_lshlrev_b32 v0, 4, v1\n"
"  s_lshl_b32 s51, s50, 15\n"
"  s_add_u32 s51, s51, 32768\n"
"  s_mov_b32 s52, %3\n"
"  s_mov_b32 s53, %4\n"
"  s_lshl_b32 s49, s50, 13\n"
"  s_lshl_b32 s54, s53, 18\n"
"  s_add_u32 s54, s54, s49\n"
"  s_add_u32 s54, s54, 3145728\n"
"  s_mov_b32 s55, s49\n"
"  s_add_u32 m0, s55, 0\n"
"  s_add_u32 s48, s54, 0\n"
"  buffer_load_dwordx4 v0, s[40:43], s48 offen lds\n"
"  s_add_u32 m0, s55, 1024\n"
"  s_add_u32 s48, s54, 1024\n"
"  buffer_load_dwordx4 v0, s[40:43], s48 offen lds\n"
"  s_add_u32 m0, s55, 2048\n"
"  s_add_u32 s48, s54, 2048\n"
"  buffer_load_dwordx4 v0, s[40:43], s48 offen lds\n"
"  s_add_u32 m0, s55, 3072\n"
"  s_add_u32 s48, s54, 3072\n"
"  buffer_load_dwordx4 v0, s[40:43], s48 offen lds\n"
"  s_add_u32 m0, s55, 4096\n"
"  s_add_u32 s48, s54, 4096\n"
"  buffer_load_dwordx4 v0, s[40:43], s48 offen lds\n"
"  s_add_u32 m0, s55, 5120\n"
"  s_add_u32 s48, s54, 5120\n"
"  buffer_load_dwordx4 v0, s[40:43], s48 offen lds\n"
"  s_add_u32 m0, s55, 6144\n"
"  s_add_u32 s48, s54, 6144\n"
"  buffer_load_dwordx4 v0, s[40:43], s48 offen lds\n"
"  s_add_u32 m0, s55, 7168\n"
"  s_add_u32 s48, s54, 7168\n"
"  buffer_load_dwordx4 v0, s[40:43], s48 offen lds\n"
"  v_lshrrev_b32 v2, 2, v1\n"
"  v_mul_u32_u24 v2, 3584, v2\n"
"  v_and_b32 v3, 0x30, v0\n"
"  v_add_u32 v2, v2, v3\n"
"  v_lshrrev_b32 v4, 4, v1\n"
"  v_mul_u32_u24 v4, 512, v4\n"
"  v_bfe_u32 v5, v1, 2, 2\n"
"  v_lshl_add_u32 v4, v5, 6, v4\n"
"  v_and_b32 v5, 3, v1\n"
"  v_lshl_add_u32 v4, v5, 3, v4\n"
"  v_add_u32 v4, s51, v4\n"
"  s_lshl_b32 s58, s52, 8\n"
"  s_lshl_b32 s48, s50, 6\n"
"  s_add_u32 s58, s58, s48\n"
"  s_mov_b64 s[44:45], %1\n"
"  s_mov_b32 s46, 1835008\n"
"  v_add_u32 v113, 56320, v2\n"
"  v_add_u32 v114, 112640, v2\n"
"  v_add_u32 v115, 168960, v2\n"
"  s_add_u32 m0, s51, 0\n"
"  s_add_u32 s48, s58, 0\n"
"  buffer_load_dwordx4 v2, s[44:47], s48 offen lds\n"
"  buffer_load_dwordx4 v113, s[44:47], s48 offen offset:1024 lds\n"
"  buffer_load_dwordx4 v114, s[44:47], s48 offen offset:2048 lds\n"
"  buffer_load_dwordx4 v115, s[44:47], s48 offen offset:3072 lds\n"
"  s_add_u32 m0, s51, 4096\n"
"  s_add_u32 s48, s58, 229376\n"
"  buffer_load_dwordx4 v2, s[44:47], s48 offen lds\n"
"  buffer_load_dwordx4 v113, s[44:47], s48 offen offset:1024 lds\n"
"  buffer_load_dwordx4 v114, s[44:47], s48 offen offset:2048 lds\n"
"  buffer_load_dwordx4 v115, s[44:47], s48 offen offset:3072 lds\n"
"  s_add_u32 m0, s51, 8192\n"
"  s_add_u32 s48, s58, 458752\n"
"  buffer_load_dwordx4 v2, s[44:47], s48 offen lds\n"
"  buffer_load_dwordx4 v113, s[44:47], s48 offen offset:1024 lds\n"
"  buffer_load_dwordx4 v114, s[44:47], s48 offen offset:2048 lds\n"
"  buffer_load_dwordx4 v115, s[44:47], s48 offen offset:3072 lds\n"
"  s_add_u32 m0, s51, 12288\n"
"  s_add_u32 s48, s58, 688128\n"
"  buffer_load_dwordx4 v2, s[44:47], s48 offen lds\n"
"  buffer_load_dwordx4 v113, s[44:47], s48 offen offset:1024 lds\n"
"  buffer_load_dwordx4 v114, s[44:47], s48 offen offset:2048 lds\n"
"  buffer_load_dwordx4 v115, s[44:47], s48 offen offset:3072 lds\n"
"  s_add_u32 m0, s51, 16384\n"
"  s_add_u32 s48, s58, 917504\n"
"  buffer_load_dwordx4 v2, s[44:47], s48 offen lds\n"
"  buffer_load_dwordx4 v113, s[44:47], s48 offen offset:1024 lds\n"
"  buffer_load_dwordx4 v114, s[44:47], s48 offen offset:2048 lds\n"
"  buffer_load_dwordx4 v115, s[44:47], s48 offen offset:3072 lds\n"
"  s_add_u32 m0, s51, 20480\n"
"  s_add_u32 s48, s58, 1146880\n"
"  buffer_load_dwordx4 v2, s[44:47], s48 offen lds\n"
"  buffer_load_dwordx4 v113, s[44:47], s48 offen offset:1024 lds\n"
"  buffer_load_dwordx4 v114, s[44:47], s48 offen offset:2048 lds\n"
"  buffer_load_dwordx4 v115, s[44:47], s48 offen offset:3072 lds\n"
"  s_add_u32 m0, s51, 24576\n"
"  s_add_u32 s48, s58, 1376256\n"
"  buffer_load_dwordx4 v2, s[44:47], s48 offen lds\n"
"  buffer_load_dwordx4 v113, s[44:47], s48 offen offset:1024 lds\n"
"  buffer_load_dwordx4 v114, s[44:47], s48 offen offset:2048 lds\n"
"  buffer_load_dwordx4 v115, s[44:47], s48 offen offset:3072 lds\n"
"  s_add_u32 m0, s51, 28672\n"
"  s_add_u32 s48, s58, 1605632\n"
"  buffer_load_dwordx4 v2, s[44:47], s48 offen lds\n"
"  buffer_load_dwordx4 v113, s[44:47], s48 offen offset:1024 lds\n"
"  buffer_load_dwordx4 v114, s[44:47], s48 offen offset:2048 lds\n"
"  buffer_load_dwordx4 v115, s[44:47], s48 offen offset:3072 lds\n"
"  s_waitcnt vmcnt(28)\n"
"  ds_read_b64_tr_b16 a[0:1], v4 offset:0\n"
"  ds_read_b64_tr_b16 a[2:3], v4 offset:256\n"
"  ds_read_b64_tr_b16 a[4:5], v4 offset:32\n"
"  ds_read_b64_tr_b16 a[6:7], v4 offset:288\n"
"  ds_read_b64_tr_b16 a[8:9], v4 offset:2048\n"
"  ds_read_b64_tr_b16 a[10:11], v4 offset:2304\n"
"  ds_read_b64_tr_b16 a[12:13], v4 offset:2080\n"
"  ds_read_b64_tr_b16 a[14:15], v4 offset:2336\n"
"  s_waitcnt vmcnt(24)\n"
"  ds_read_b64_tr_b16 a[16:17], v4 offset:4096\n"
"  ds_read_b64_tr_b16 a[18:19], v4 offset:4352\n"
"  ds_read_b64_tr_b16 a[20:21], v4 offset:4128\n"
"  ds_read_b64_tr_b16 a[22:23], v4 offset:4384\n"
"  s_waitcnt lgkmcnt(4)\n"
"  s_mov_b64 s[44:45], %2\n"
"  s_mov_b32 s46, 1835008\n"
"  s_add_u32 m0, s51, 0\n"
"  s_add_u32 s48, s58, 0\n"
"  buffer_load_dwordx4 v2, s[44:47], s48 offen lds\n"
"  buffer_load_dwordx4 v113, s[44:47], s48 offen offset:1024 lds\n"
"  buffer_load_dwordx4 v114, s[44:47], s48 offen offset:2048 lds\n"
"  buffer_load_dwordx4 v115, s[44:47], s48 offen offset:3072 lds\n"
"  ds_read_b64_tr_b16 a[24:25], v4 offset:6144\n"
"  ds_read_b64_tr_b16 a[26:27], v4 offset:6400\n"
"  ds_read_b64_tr_b16 a[28:29], v4 offset:6176\n"
"  ds_read_b64_tr_b16 a[30:31], v4 offset:6432\n"
"  s_waitcnt vmcnt(24)\n"
"  ds_read_b64_tr_b16 a[32:33], v4 offset:8192\n"
"  ds_read_b64_tr_b16 a[34:35], v4 offset:8448\n"
"  ds_read_b64_tr_b16 a[36:37], v4 offset:8224\n"
"  ds_read_b64_tr_b16 a[38:39], v4 offset:8480\n"
"  s_waitcnt lgkmcnt(4)\n"
"  s_add_u32 m0, s51, 4096\n"
"  s_add_u32 s48, s58, 229376\n"
"  buffer_load_dwordx4 v2, s[44:47], s48 offen lds\n"
"  buffer_load_dwordx4 v113, s[44:47], s48 offen offset:1024 lds\n"
"  buffer_load_dwordx4 v114, s[44:47], s48 offen offset:2048 lds\n"
"  buffer_load_dwordx4 v115, s[44:47], s48 offen offset:3072 lds\n"
"  ds_read_b64_tr_b16 a[40:41], v4 offset:10240\n"
"  ds_read_b64_tr_b16 a[42:43], v4 offset:10496\n"
"  ds_read_b64_tr_b16 a[44:45], v4 offset:10272\n"
"  ds_read_b64_tr_b16 a[46:47], v4 offset:10528\n"
"  s_waitcnt vmcnt(24)\n"
"  ds_read_b64_tr_b16 a[48:49], v4 offset:12288\n"
"  ds_read_b64_tr_b16 a[50:51], v4 offset:12544\n"
"  ds_read_b64_tr_b16 a[52:53], v4 offset:12320\n"
"  ds_read_b64_tr_b16 a[54:55], v4 offset:12576\n"
"  s_waitcnt lgkmcnt(4)\n"
"  s_add_u32 m0, s51, 8192\n"
"  s_add_u32 s48, s58, 458752\n"
"  buffer_load_dwordx4 v2, s[44:47], s48 offen lds\n"
"  buffer_load_dwordx4 v113, s[44:47], s48 offen offset:1024 lds\n"
"  buffer_load_dwordx4 v114, s[44:47], s48 offen offset:2048 lds\n"
"  buffer_load_dwordx4 v115, s[44:47], s48 offen offset:3072 lds\n"
"  ds_read_b64_tr_b16 a[56:57], v4 offset:14336\n"
"  ds_read_b64_tr_b16 a[58:59], v4 offset:14592\n"
"  ds_read_b64_tr_b16 a[60:61], v4 offset:14368\n"
"  ds_read_b64_tr_b16 a[62:63], v4 offset:14624\n"
"  s_waitcnt vmcnt(24)\n"
"  ds_read_b64_tr_b16 a[64:65], v4 offset:16384\n"
"  ds_read_b64_tr_b16 a[66:67], v4 offset:16640\n"
"  ds_read_b64_tr_b16 a[68:69], v4 offset:16416\n"
"  ds_read_b64_tr_b16 a[70:71], v4 offset:16672\n"
"  s_waitcnt lgkmcnt(4)\n"
"  s_add_u32 m0, s51, 12288\n"
"  s_add_u32 s48, s58, 688128\n"
"  buffer_load_dwordx4 v2, s[44:47], s48 offen lds\n"
"  buffer_load_dwordx4 v113, s[44:47], s48 offen offset:1024 lds\n"
"  buffer_load_dwordx4 v114, s[44:47], s48 offen offset:2048 lds\n"
"  buffer_load_dwordx4 v115, s[44:47], s48 offen offset:3072 lds\n"
"  ds_read_b64_tr_b16 a[72:73], v4 offset:18432\n"
"  ds_read_b64_tr_b16 a[74:75], v4 offset:18688\n"
"  ds_read_b64_tr_b16 a[76:77], v4 offset:18464\n"
"  ds_read_b64_tr_b16 a[78:79], v4 offset:18720\n"
"  s_waitcnt vmcnt(24)\n"
"  ds_read_b64_tr_b16 a[80:81], v4 offset:20480\n"
"  ds_read_b64_tr_b16 a[82:83], v4 offset:20736\n"
"  ds_read_b64_tr_b16 a[84:85], v4 offset:20512\n"
"  ds_read_b64_tr_b16 a[86:87], v4 offset:20768\n"
"  s_waitcnt lgkmcnt(4)\n"
"  s_add_u32 m0, s51, 16384\n"
"  s_add_u32 s48, s58, 917504\n"
"  buffer_load_dwordx4 v2, s[44:47], s48 offen lds\n"
"  buffer_load_dwordx4 v113, s[44:47], s48 offen offset:1024 lds\n"
"  buffer_load_dwordx4 v114, s[44:47], s48 offen offset:2048 lds\n"
"  buffer_load_dwordx4 v115, s[44:47], s48 offen offset:3072 lds\n"
"  ds_read_b64_tr_b16 a[88:89], v4 offset:22528\n"
"  ds_read_b64_tr_b16 a[90:91], v4 offset:22784\n"
"  ds_read_b64_tr_b16 a[92:93], v4 offset:22560\n"
"  ds_read_b64_tr_b16 a[94:95], v4 offset:22816\n"
"  s_waitcnt vmcnt(24)\n"
"  ds_read_b64_tr_b16 a[96:97], v4 offset:24576\n"
"  ds_read_b64_tr_b16 a[98:99], v4 offset:24832\n"
"  ds_read_b64_tr_b16 a[100:101], v4 offset:24608\n"
"  ds_read_b64_tr_b16 a[102:103], v4 offset:24864\n"
"  s_waitcnt lgkmcnt(4)\n"
"  s_add_u32 m0, s51, 20480\n"
"  s_add_u32 s48, s58, 1146880\n"
"  buffer_load_dwordx4 v2, s[44:47], s48 offen lds\n"
"  buffer_load_dwordx4 v113, s[44:47], s48 offen offset:1024 lds\n"
"  buffer_load_dwordx4 v114, s[44:47], s48 offen offset:2048 lds\n"
"  buffer_load_dwordx4 v115, s[44:47], s48 offen offset:3072 lds\n"
"  ds_read_b64_tr_b16 a[104:105], v4 offset:26624\n"
"  ds_read_b64_tr_b16 a[106:107], v4 offset:26880\n"
"  ds_read_b64_tr_b16 a[108:109], v4 offset:26656\n"
"  ds_read_b64_tr_b16 a[110:111], v4 offset:26912\n"
"  s_waitcnt vmcnt(24)\n"
"  ds_read_b64_tr_b16 a[112:113], v4 offset:28672\n"
"  ds_read_b64_tr_b16 a[114:115], v4 offset:28928\n"
"  ds_read_b64_tr_b16 a[116:117], v4 offset:28704\n"
"  ds_read_b64_tr_b16 a[118:119], v4 offset:28960\n"
"  s_waitcnt lgkmcnt(4)\n"
"  s_add_u32 m0, s51, 24576\n"
"  s_add_u32 s48, s58, 1376256\n"
"  buffer_load_dwordx4 v2, s[44:47], s48 offen lds\n"
"  buffer_load_dwordx4 v113, s[44:47], s48 offen offset:1024 lds\n"
"  buffer_load_dwordx4 v114, s[44:47], s48 offen offset:2048 lds\n"
"  buffer_load_dwordx4 v115, s[44:47], s48 offen offset:3072 lds\n"
"  ds_read_b64_tr_b16 a[120:121], v4 offset:30720\n"
"  ds_read_b64_tr_b16 a[122:123], v4 offset:30976\n"
"  ds_read_b64_tr_b16 a[124:125], v4 offset:30752\n"
"  ds_read_b64_tr_b16 a[126:127], v4 offset:31008\n"
"  s_waitcnt vmcnt(24)\n"
"  ds_read_b64_tr_b16 a[128:129], v4 offset:0\n"
"  ds_read_b64_tr_b16 a[130:131], v4 offset:256\n"
"  ds_read_b64_tr_b16 a[132:133], v4 offset:32\n"
"  ds_read_b64_tr_b16 a[134:135], v4 offset:288\n"
"  s_waitcnt lgkmcnt(4)\n"
"  s_add_u32 m0, s51, 28672\n"
"  s_add_u32 s48, s58, 1605632\n"
"  buffer_load_dwordx4 v2, s[44:47], s48 offen lds\n"
"  buffer_load_dwordx4 v113, s[44:47], s48 offen offset:1024 lds\n"
"  buffer_load_dwordx4 v114, s[44:47], s48 offen offset:2048 lds\n"
"  buffer_load_dwordx4 v115, s[44:47], s48 offen offset:3072 lds\n"
"  ds_read_b64_tr_b16 a[136:137], v4 offset:2048\n"
"  ds_read_b64_tr_b16 a[138:139], v4 offset:2304\n"
"  ds_read_b64_tr_b16 a[140:141], v4 offset:2080\n"
"  ds_read_b64_tr_b16 a[142:143], v4 offset:2336\n"
"  s_waitcnt vmcnt(24)\n"
"  ds_read_b64_tr_b16 a[144:145], v4 offset:4096\n"
"  ds_read_b64_tr_b16 a[146:147], v4 offset:4352\n"
"  ds_read_b64_tr_b16 a[148:149], v4 offset:4128\n"
"  ds_read_b64_tr_b16 a[150:151], v4 offset:4384\n"
"  s_waitcnt lgkmcnt(4)\n"
"  ds_read_b64_tr_b16 a[152:153], v4 offset:6144\n"
"  ds_read_b64_tr_b16 a[154:155], v4 offset:6400\n"
"  ds_read_b64_tr_b16 a[156:157], v4 offset:6176\n"
"  ds_read_b64_tr_b16 a[158:159], v4 offset:6432\n"
"  s_waitcnt vmcnt(20)\n"
"  ds_read_b64_tr_b16 a[160:161], v4 offset:8192\n"
"  ds_read_b64_tr_b16 a[162:163], v4 offset:8448\n"
"  ds_read_b64_tr_b16 a[164:165], v4 offset:8224\n"
"  ds_read_b64_tr_b16 a[166:167], v4 offset:8480\n"
"  s_waitcnt lgkmcnt(4)\n"
"  ds_read_b64_tr_b16 a[168:169], v4 offset:10240\n"
"  ds_read_b64_tr_b16 a[170:171], v4 offset:10496\n"
"  ds_read_b64_tr_b16 a[172:173], v4 offset:10272\n"
"  ds_read_b64_tr_b16 a[174:175], v4 offset:10528\n"
"  s_waitcnt vmcnt(16)\n"
"  ds_read_b64_tr_b16 a[176:177], v4 offset:12288\n"
"  ds_read_b64_tr_b16 a[178:179], v4 offset:12544\n"
"  ds_read_b64_tr_b16 a[180:181], v4 offset:12320\n"
"  ds_read_b64_tr_b16 a[182:183], v4 offset:12576\n"
"  s_waitcnt lgkmcnt(4)\n"
"  ds_read_b64_tr_b16 a[184:185], v4 offset:14336\n"
"  ds_read_b64_tr_b16 a[186:187], v4 offset:14592\n"
"  ds_read_b64_tr_b16 a[188:189], v4 offset:14368\n"
"  ds_read_b64_tr_b16 a[190:191], v4 offset:14624\n"
"  s_waitcnt vmcnt(12)\n"
"  ds_read_b64_tr_b16 a[192:193], v4 offset:16384\n"
"  ds_read_b64_tr_b16 a[194:195], v4 offset:16640\n"
"  ds_read_b64_tr_b16 a[196:197], v4 offset:16416\n"
"  ds_read_b64_tr_b16 a[198:199], v4 offset:16672\n"
"  s_waitcnt lgkmcnt(4)\n"
"  ds_read_b64_tr_b16 a[200:201], v4 offset:18432\n"
"  ds_read_b64_tr_b16 a[202:203], v4 offset:18688\n"
"  ds_read_b64_tr_b16 a[204:205], v4 offset:18464\n"
"  ds_read_b64_tr_b16 a[206:207], v4 offset:18720\n"
"  s_waitcnt vmcnt(8)\n"
"  ds_read_b64_tr_b16 a[208:209], v4 offset:20480\n"
"  ds_read_b64_tr_b16 a[210:211], v4 offset:20736\n"
"  ds_read_b64_tr_b16 a[212:213], v4 offset:20512\n"
"  ds_read_b64_tr_b16 a[214:215], v4 offset:20768\n"
"  s_waitcnt lgkmcnt(4)\n"
"  ds_read_b64_tr_b16 a[216:217], v4 offset:22528\n"
"  ds_read_b64_tr_b16 a[218:219], v4 offset:22784\n"
"  ds_read_b64_tr_b16 a[220:221], v4 offset:22560\n"
"  ds_read_b64_tr_b16 a[222:223], v4 offset:22816\n"
"  s_waitcnt vmcnt(4)\n"
"  ds_read_b64_tr_b16 a[224:225], v4 offset:24576\n"
"  ds_read_b64_tr_b16 a[226:227], v4 offset:24832\n"
"  ds_read_b64_tr_b16 a[228:229], v4 offset:24608\n"
"  ds_read_b64_tr_b16 a[230:231], v4 offset:24864\n"
"  s_waitcnt lgkmcnt(4)\n"
"  ds_read_b64_tr_b16 a[232:233], v4 offset:26624\n"
"  ds_read_b64_tr_b16 a[234:235], v4 offset:26880\n"
"  ds_read_b64_tr_b16 a[236:237], v4 offset:26656\n"
"  ds_read_b64_tr_b16 a[238:239], v4 offset:26912\n"
"  s_waitcnt vmcnt(0)\n"
"  ds_read_b64_tr_b16 a[240:241], v4 offset:28672\n"
"  ds_read_b64_tr_b16 a[242:243], v4 offset:28928\n"
"  ds_read_b64_tr_b16 a[244:245], v4 offset:28704\n"
"  ds_read_b64_tr_b16 a[246:247], v4 offset:28960\n"
"  s_waitcnt lgkmcnt(4)\n"
"  ds_read_b64_tr_b16 a[248:249], v4 offset:30720\n"
"  ds_read_b64_tr_b16 a[250:251], v4 offset:30976\n"
"  ds_read_b64_tr_b16 a[252:253], v4 offset:30752\n"
"  ds_read_b64_tr_b16 a[254:255], v4 offset:31008\n"
"  s_waitcnt lgkmcnt(0)\n"
"  s_mul_i32 s56, s53, 917504\n"
"  s_lshl_b32 s48, s52, 12\n"
"  s_add_u32 s56, s56, s48\n"
"  s_lshl_b32 s48, s50, 10\n"
"  s_add_u32 s56, s56, s48\n"
"  s_add_u32 s56, s56, 117440512\n"
"  s_mov_b32 s57, 0x7f000000\n"
"  s_mov_b32 s59, 0\n"
"  s_mov_b32 s61, 0xbdd2d3e8\n"
"  v_mov_b32 v2, v0\n"
"  v_mov_b32 v3, 0xc0135761\n"
"  s_barrier\n"
"L_p2%=:\n"
"  s_cmp_lt_u32 s59, 7\n"
"  s_cselect_b32 s49, 32768, 0\n"
"  s_add_u32 s54, s54, s49\n"
"  s_xor_b32 s55, s55, 32768\n"
"  ds_read_b128 v[48:51], v2 offset:0\n"
"  ds_read_b128 v[52:55], v2 offset:1024\n"
"  ds_read_b128 v[56:59], v2 offset:2048\n"
"  s_waitcnt lgkmcnt(2)\n"
"  v_mfma_f32_16x16x32_bf16 v[16:19], a[0:3], v[48:51], 0\n"
"  ds_read_b128 v[60:63], v2 offset:3072\n"
"  v_mfma_f32_16x16x32_bf16 v[20:23], a[4:7], v[48:51], 0\n"
"  v_mfma_f32_16x16x32_bf16 v[24:27], a[128:131], v[48:51], 0\n"
"  v_mfma_f32_16x16x32_bf16 v[28:31], a[132:135], v[48:51], 0\n"
"  s_waitcnt lgkmcnt(2)\n"
"  v_mfma_f32_16x16x32_bf16 v[16:19], a[8:11], v[52:55], v[16:19]\n"
"  ds_read_b128 v[48:51], v2 offset:4096\n"
"  v_mfma_f32_16x16x32_bf16 v[20:23], a[12:15], v[52:55], v[20:23]\n"
"  v_mfma_f32_16x16x32_bf16 v[24:27], a[136:139], v[52:55], v[24:27]\n"
"  v_mfma_f32_16x16x32_bf16 v[28:31], a[140:143], v[52:55], v[28:31]\n"
"  s_waitcnt lgkmcnt(2)\n"
"  v_mfma_f32_16x16x32_bf16 v[16:19], a[16:19], v[56:59], v[16:19]\n"
"  ds_read_b128 v[52:55], v2 offset:5120\n"
"  v_mul_f32 v64, v32, v32\n"
"  v_mul_f32 v65, v33, v33\n"
"  v_mfma_f32_16x16x32_bf16 v[20:23], a[20:23], v[56:59], v[20:23]\n"
"  v_mul_f32 v66, v34, v34\n"
"  v_mul_f32 v67, v35, v35\n"
"  v_mfma_f32_16x16x32_bf16 v[24:27], a[144:147], v[56:59], v[24:27]\n"
"  v_mul_f32 v68, v36, v36\n"
"  v_mul_f32 v69, v37, v37\n"
"  v_mfma_f32_16x16x32_bf16 v[28:31], a[148:151], v[56:59], v[28:31]\n"
"  v_mul_f32 v70, v38, v38\n"
"  v_mul_f32 v71, v39, v39\n"
"  s_waitcnt lgkmcnt(2)\n"
"  v_mfma_f32_16x16x32_bf16 v[16:19], a[24:27], v[60:63], v[16:19]\n"
"  ds_read_b128 v[56:59], v2 offset:6144\n"
"  v_fma_f32 v64, v64, s61, v3\n"
"  v_fma_f32 v65, v65, s61, v3\n"
"  v_mfma_f32_16x16x32_bf16 v[20:23], a[28:31], v[60:63], v[20:23]\n"
"  v_fma_f32 v66, v66, s61, v3\n"
"  v_fma_f32 v67, v67, s61, v3\n"
"  v_mfma_f32_16x16x32_bf16 v[24:27], a[152:155], v[60:63], v[24:27]\n"
"  v_fma_f32 v68, v68, s61, v3\n"
"  v_fma_f32 v69, v69, s61, v3\n"
"  v_mfma_f32_16x16x32_bf16 v[28:31], a[156:159], v[60:63], v[28:31]\n"
"  v_fma_f32 v70, v70, s61, v3\n"
"  v_fma_f32 v71, v71, s61, v3\n"
"  s_waitcnt lgkmcnt(2)\n"
"  v_mfma_f32_16x16x32_bf16 v[16:19], a[32:35], v[48:51], v[16:19]\n"
"  ds_read_b128 v[60:63], v2 offset:7168\n"
"  v_mul_f32 v64, v64, v32\n"
"  v_mul_f32 v65, v65, v33\n"
"  v_mfma_f32_16x16x32_bf16 v[20:23], a[36:39], v[48:51], v[20:23]\n"
"  v_mul_f32 v66, v66, v34\n"
"  v_mul_f32 v67, v67, v35\n"
"  v_mfma_f32_16x16x32_bf16 v[24:27], a[160:163], v[48:51], v[24:27]\n"
"  v_mul_f32 v68, v68, v36\n"
"  v_mul_f32 v69, v69, v37\n"
"  v_mfma_f32_16x16x32_bf16 v[28:31], a[164:167], v[48:51], v[28:31]\n"
"  v_mul_f32 v70, v70, v38\n"
"  v_mul_f32 v71, v71, v39\n"
"  s_waitcnt lgkmcnt(2)\n"
"  v_mfma_f32_16x16x32_bf16 v[16:19], a[40:43], v[52:55], v[16:19]\n"
"  ds_read_b128 v[48:51], v2 offset:8192\n"
"  v_exp_f32 v64, v64\n"
"  v_exp_f32 v65, v65\n"
"  v_mfma_f32_16x16x32_bf16 v[20:23], a[44:47], v[52:55], v[20:23]\n"
"  v_exp_f32 v66, v66\n"
"  v_exp_f32 v67, v67\n"
"  v_mfma_f32_16x16x32_bf16 v[24:27], a[168:171], v[52:55], v[24:27]\n"
"  v_exp_f32 v68, v68\n"
"  v_exp_f32 v69, v69\n"
"  v_mfma_f32_16x16x32_bf16 v[28:31], a[172:175], v[52:55], v[28:31]\n"
"  v_exp_f32 v70, v70\n"
"  v_exp_f32 v71, v71\n"
"  s_waitcnt lgkmcnt(2)\n"
"  v_mfma_f32_16x16x32_bf16 v[16:19], a[48:51], v[56:59], v[16:19]\n"
"  ds_read_b128 v[52:55], v2 offset:9216\n"
"  v_add_f32 v64, 1.0, v64\n"
"  v_add_f32 v65, 1.0, v65\n"
"  v_mfma_f32_16x16x32_bf16 v[20:23], a[52:55], v[56:59], v[20:23]\n"
"  v_add_f32 v66, 1.0, v66\n"
"  v_add_f32 v67, 1.0, v67\n"
"  v_mfma_f32_16x16x32_bf16 v[24:27], a[176:179], v[56:59], v[24:27]\n"
"  v_add_f32 v68, 1.0, v68\n"
"  v_add_f32 v69, 1.0, v69\n"
"  v_mfma_f32_16x16x32_bf16 v[28:31], a[180:183], v[56:59], v[28:31]\n"
"  v_add_f32 v70, 1.0, v70\n"
"  v_add_f32 v71, 1.0, v71\n"
"  s_waitcnt lgkmcnt(2)\n"
"  v_mfma_f32_16x16x32_bf16 v[16:19], a[56:59], v[60:63], v[16:19]\n"
"  ds_read_b128 v[56:59], v2 offset:10240\n"
"  v_rcp_f32 v64, v64\n"
"  v_rcp_f32 v65, v65\n"
"  v_mfma_f32_16x16x32_bf16 v[20:23], a[60:63], v[60:63], v[20:23]\n"
"  v_rcp_f32 v66, v66\n"
"  v_rcp_f32 v67, v67\n"
"  v_mfma_f32_16x16x32_bf16 v[24:27], a[184:187], v[60:63], v[24:27]\n"
"  v_rcp_f32 v68, v68\n"
"  v_rcp_f32 v69, v69\n"
"  v_mfma_f32_16x16x32_bf16 v[28:31], a[188:191], v[60:63], v[28:31]\n"
"  v_rcp_f32 v70, v70\n"
"  v_rcp_f32 v71, v71\n"
"  s_waitcnt lgkmcnt(2)\n"
"  v_mfma_f32_16x16x32_bf16 v[16:19], a[64:67], v[48:51], v[16:19]\n"
"  ds_read_b128 v[60:63], v2 offset:11264\n"
"  v_mul_f32 v64, v64, v32\n"
"  v_mul_f32 v65, v65, v33\n"
"  v_mfma_f32_16x16x32_bf16 v[20:23], a[68:71], v[48:51], v[20:23]\n"
"  v_mul_f32 v66, v66, v34\n"
"  v_mul_f32 v67, v67, v35\n"
"  v_mfma_f32_16x16x32_bf16 v[24:27], a[192:195], v[48:51], v[24:27]\n"
"  v_mul_f32 v68, v68, v36\n"
"  v_mul_f32 v69, v69, v37\n"
"  v_mfma_f32_16x16x32_bf16 v[28:31], a[196:199], v[48:51], v[28:31]\n"
"  v_mul_f32 v70, v70, v38\n"
"  v_mul_f32 v71, v71, v39\n"
"  s_waitcnt lgkmcnt(2)\n"
"  v_mfma_f32_16x16x32_bf16 v[16:19], a[72:75], v[52:55], v[16:19]\n"
"  ds_read_b128 v[48:51], v2 offset:12288\n"
"  v_mul_f32 v64, v64, v40\n"
"  v_mul_f32 v65, v65, v41\n"
"  v_mfma_f32_16x16x32_bf16 v[20:23], a[76:79], v[52:55], v[20:23]\n"
"  v_mul_f32 v66, v66, v42\n"
"  v_mul_f32 v67, v67, v43\n"
"  v_mfma_f32_16x16x32_bf16 v[24:27], a[200:203], v[52:55], v[24:27]\n"
"  v_mul_f32 v68, v68, v44\n"
"  v_mul_f32 v69, v69, v45\n"
"  v_mfma_f32_16x16x32_bf16 v[28:31], a[204:207], v[52:55], v[28:31]\n"
"  v_mul_f32 v70, v70, v46\n"
"  v_mul_f32 v71, v71, v47\n"
"  s_waitcnt lgkmcnt(2)\n"
"  v_mfma_f32_16x16x32_bf16 v[16:19], a[80:83], v[56:59], v[16:19]\n"
"  ds_read_b128 v[52:55], v2 offset:13312\n"
"  v_cvt_pk_bf16_f32 v80, v64, v65\n"
"  v_cvt_pk_bf16_f32 v81, v66, v67\n"
"  v_mfma_f32_16x16x32_bf16 v[20:23], a[84:87], v[56:59], v[20:23]\n"
"  v_cvt_pk_bf16_f32 v82, v68, v69\n"
"  v_cvt_pk_bf16_f32 v83, v70, v71\n"
"  v_mfma_f32_16x16x32_bf16 v[24:27], a[208:211], v[56:59], v[24:27]\n"
"  buffer_store_dwordx4 v[80:83], v0, s[40:43], s57 offen sc1\n"
"  s_add_u32 m0, s55, 0\n"
"  s_add_u32 s49, s54, 0\n"
"  buffer_load_dwordx4 v0, s[40:43], s49 offen lds\n"
"  v_mfma_f32_16x16x32_bf16 v[28:31], a[212:215], v[56:59], v[28:31]\n"
"  s_waitcnt lgkmcnt(2)\n"
"  v_mfma_f32_16x16x32_bf16 v[16:19], a[88:91], v[60:63], v[16:19]\n"
"  ds_read_b128 v[56:59], v2 offset:14336\n"
"  s_add_u32 m0, s55, 1024\n"
"  s_add_u32 s49, s54, 1024\n"
"  buffer_load_dwordx4 v0, s[40:43], s49 offen lds\n"
"  v_mfma_f32_16x16x32_bf16 v[20:23], a[92:95], v[60:63], v[20:23]\n"
"  v_mfma_f32_16x16x32_bf16 v[24:27], a[216:219], v[60:63], v[24:27]\n"
"  s_add_u32 m0, s55, 2048\n"
"  s_add_u32 s49, s54, 2048\n"
"  buffer_load_dwordx4 v0, s[40:43], s49 offen lds\n"
"  v_mfma_f32_16x16x32_bf16 v[28:31], a[220:223], v[60:63], v[28:31]\n"
"  s_waitcnt lgkmcnt(2)\n"
"  v_mfma_f32_16x16x32_bf16 v[16:19], a[96:99], v[48:51], v[16:19]\n"
"  ds_read_b128 v[60:63], v2 offset:15360\n"
"  s_add_u32 m0, s55, 3072\n"
"  s_add_u32 s49, s54, 3072\n"
"  buffer_load_dwordx4 v0, s[40:43], s49 offen lds\n"
"  v_mfma_f32_16x16x32_bf16 v[20:23], a[100:103], v[48:51], v[20:23]\n"
"  v_mfma_f32_16x16x32_bf16 v[24:27], a[224:227], v[48:51], v[24:27]\n"
"  s_add_u32 m0, s55, 4096\n"
"  s_add_u32 s49, s54, 4096\n"
"  buffer_load_dwordx4 v0, s[40:43], s49 offen lds\n"
"  v_mfma_f32_16x16x32_bf16 v[28:31], a[228:231], v[48:51], v[28:31]\n"
"  s_waitcnt lgkmcnt(2)\n"
"  v_mfma_f32_16x16x32_bf16 v[16:19], a[104:107], v[52:55], v[16:19]\n"
"  ds_read_b128 v[48:51], v2 offset:16384\n"
"  s_add_u32 m0, s55, 5120\n"
"  s_add_u32 s49, s54, 5120\n"
"  buffer_load_dwordx4 v0, s[40:43], s49 offen lds\n"
"  v_mfma_f32_16x16x32_bf16 v[20:23], a[108:111], v[52:55], v[20:23]\n"
"  v_mfma_f32_16x16x32_bf16 v[24:27], a[232:235], v[52:55], v[24:27]\n"
"  s_add_u32 m0, s55, 6144\n"
"  s_add_u32 s49, s54, 6144\n"
"  buffer_load_dwordx4 v0, s[40:43], s49 offen lds\n"
"  v_mfma_f32_16x16x32_bf16 v[28:31], a[236:239], v[52:55], v[28:31]\n"
"  s_waitcnt lgkmcnt(2)\n"
"  v_mfma_f32_16x16x32_bf16 v[16:19], a[112:115], v[56:59], v[16:19]\n"
"  ds_read_b128 v[52:55], v2 offset:17408\n"
"  s_add_u32 m0, s55, 7168\n"
"  s_add_u32 s49, s54, 7168\n"
"  buffer_load_dwordx4 v0, s[40:43], s49 offen lds\n"
"  v_mfma_f32_16x16x32_bf16 v[20:23], a[116:119], v[56:59], v[20:23]\n"
"  v_mfma_f32_16x16x32_bf16 v[24:27], a[240:243], v[56:59], v[24:27]\n"
"  v_mfma_f32_16x16x32_bf16 v[28:31], a[244:247], v[56:59], v[28:31]\n"
"  s_waitcnt lgkmcnt(2)\n"
"  v_mfma_f32_16x16x32_bf16 v[16:19], a[120:123], v[60:63], v[16:19]\n"
"  ds_read_b128 v[56:59], v2 offset:18432\n"
"  v_mfma_f32_16x16x32_bf16 v[20:23], a[124:127], v[60:63], v[20:23]\n"
"  v_mfma_f32_16x16x32_bf16 v[24:27], a[248:251], v[60:63], v[24:27]\n"
"  v_mfma_f32_16x16x32_bf16 v[28:31], a[252:255], v[60:63], v[28:31]\n"
"  s_waitcnt lgkmcnt(2)\n"
"  v_mfma_f32_16x16x32_bf16 v[32:35], a[0:3], v[48:51], 0\n"
"  ds_read_b128 v[60:63], v2 offset:19456\n"
"  v_mfma_f32_16x16x32_bf16 v[36:39], a[4:7], v[48:51], 0\n"
"  v_mfma_f32_16x16x32_bf16 v[40:43], a[128:131], v[48:51], 0\n"
"  v_mfma_f32_16x16x32_bf16 v[44:47], a[132:135], v[48:51], 0\n"
"  s_waitcnt lgkmcnt(2)\n"
"  v_mfma_f32_16x16x32_bf16 v[32:35], a[8:11], v[52:55], v[32:35]\n"
"  ds_read_b128 v[48:51], v2 offset:20480\n"
"  v_mfma_f32_16x16x32_bf16 v[36:39], a[12:15], v[52:55], v[36:39]\n"
"  v_mfma_f32_16x16x32_bf16 v[40:43], a[136:139], v[52:55], v[40:43]\n"
"  v_mfma_f32_16x16x32_bf16 v[44:47], a[140:143], v[52:55], v[44:47]\n"
"  s_waitcnt lgkmcnt(2)\n"
"  v_mfma_f32_16x16x32_bf16 v[32:35], a[16:19], v[56:59], v[32:35]\n"
"  ds_read_b128 v[52:55], v2 offset:21504\n"
"  v_mul_f32 v64, v16, v16\n"
"  v_mul_f32 v65, v17, v17\n"
"  v_mfma_f32_16x16x32_bf16 v[36:39], a[20:23], v[56:59], v[36:39]\n"
"  v_mul_f32 v66, v18, v18\n"
"  v_mul_f32 v67, v19, v19\n"
"  v_mfma_f32_16x16x32_bf16 v[40:43], a[144:147], v[56:59], v[40:43]\n"
"  v_mul_f32 v68, v20, v20\n"
"  v_mul_f32 v69, v21, v21\n"
"  v_mfma_f32_16x16x32_bf16 v[44:47], a[148:151], v[56:59], v[44:47]\n"
"  v_mul_f32 v70, v22, v22\n"
"  v_mul_f32 v71, v23, v23\n"
"  s_waitcnt lgkmcnt(2)\n"
"  v_mfma_f32_16x16x32_bf16 v[32:35], a[24:27], v[60:63], v[32:35]\n"
"  ds_read_b128 v[56:59], v2 offset:22528\n"
"  v_fma_f32 v64, v64, s61, v3\n"
"  v_fma_f32 v65, v65, s61, v3\n"
"  v_mfma_f32_16x16x32_bf16 v[36:39], a[28:31], v[60:63], v[36:39]\n"
"  v_fma_f32 v66, v66, s61, v3\n"
"  v_fma_f32 v67, v67, s61, v3\n"
"  v_mfma_f32_16x16x32_bf16 v[40:43], a[152:155], v[60:63], v[40:43]\n"
"  v_fma_f32 v68, v68, s61, v3\n"
"  v_fma_f32 v69, v69, s61, v3\n"
"  v_mfma_f32_16x16x32_bf16 v[44:47], a[156:159], v[60:63], v[44:47]\n"
"  v_fma_f32 v70, v70, s61, v3\n"
"  v_fma_f32 v71, v71, s61, v3\n"
"  s_waitcnt lgkmcnt(2)\n"
"  v_mfma_f32_16x16x32_bf16 v[32:35], a[32:35], v[48:51], v[32:35]\n"
"  ds_read_b128 v[60:63], v2 offset:23552\n"
"  v_mul_f32 v64, v64, v16\n"
"  v_mul_f32 v65, v65, v17\n"
"  v_mfma_f32_16x16x32_bf16 v[36:39], a[36:39], v[48:51], v[36:39]\n"
"  v_mul_f32 v66, v66, v18\n"
"  v_mul_f32 v67, v67, v19\n"
"  v_mfma_f32_16x16x32_bf16 v[40:43], a[160:163], v[48:51], v[40:43]\n"
"  v_mul_f32 v68, v68, v20\n"
"  v_mul_f32 v69, v69, v21\n"
"  v_mfma_f32_16x16x32_bf16 v[44:47], a[164:167], v[48:51], v[44:47]\n"
"  v_mul_f32 v70, v70, v22\n"
"  v_mul_f32 v71, v71, v23\n"
"  s_waitcnt lgkmcnt(2)\n"
"  v_mfma_f32_16x16x32_bf16 v[32:35], a[40:43], v[52:55], v[32:35]\n"
"  ds_read_b128 v[48:51], v2 offset:24576\n"
"  v_exp_f32 v64, v64\n"
"  v_exp_f32 v65, v65\n"
"  v_mfma_f32_16x16x32_bf16 v[36:39], a[44:47], v[52:55], v[36:39]\n"
"  v_exp_f32 v66, v66\n"
"  v_exp_f32 v67, v67\n"
"  v_mfma_f32_16x16x32_bf16 v[40:43], a[168:171], v[52:55], v[40:43]\n"
"  v_exp_f32 v68, v68\n"
"  v_exp_f32 v69, v69\n"
"  v_mfma_f32_16x16x32_bf16 v[44:47], a[172:175], v[52:55], v[44:47]\n"
"  v_exp_f32 v70, v70\n"
"  v_exp_f32 v71, v71\n"
"  s_waitcnt lgkmcnt(2)\n"
"  v_mfma_f32_16x16x32_bf16 v[32:35], a[48:51], v[56:59], v[32:35]\n"
"  ds_read_b128 v[52:55], v2 offset:25600\n"
"  v_add_f32 v64, 1.0, v64\n"
"  v_add_f32 v65, 1.0, v65\n"
"  v_mfma_f32_16x16x32_bf16 v[36:39], a[52:55], v[56:59], v[36:39]\n"
"  v_add_f32 v66, 1.0, v66\n"
"  v_add_f32 v67, 1.0, v67\n"
"  v_mfma_f32_16x16x32_bf16 v[40:43], a[176:179], v[56:59], v[40:43]\n"
"  v_add_f32 v68, 1.0, v68\n"
"  v_add_f32 v69, 1.0, v69\n"
"  v_mfma_f32_16x16x32_bf16 v[44:47], a[180:183], v[56:59], v[44:47]\n"
"  v_add_f32 v70, 1.0, v70\n"
"  v_add_f32 v71, 1.0, v71\n"
"  s_waitcnt lgkmcnt(2)\n"
"  v_mfma_f32_16x16x32_bf16 v[32:35], a[56:59], v[60:63], v[32:35]\n"
"  ds_read_b128 v[56:59], v2 offset:26624\n"
"  v_rcp_f32 v64, v64\n"
"  v_rcp_f32 v65, v65\n"
"  v_mfma_f32_16x16x32_bf16 v[36:39], a[60:63], v[60:63], v[36:39]\n"
"  v_rcp_f32 v66, v66\n"
"  v_rcp_f32 v67, v67\n"
"  v_mfma_f32_16x16x32_bf16 v[40:43], a[184:187], v[60:63], v[40:43]\n"
"  v_rcp_f32 v68, v68\n"
"  v_rcp_f32 v69, v69\n"
"  v_mfma_f32_16x16x32_bf16 v[44:47], a[188:191], v[60:63], v[44:47]\n"
"  v_rcp_f32 v70, v70\n"
"  v_rcp_f32 v71, v71\n"
"  s_waitcnt lgkmcnt(2)\n"
"  v_mfma_f32_16x16x32_bf16 v[32:35], a[64:67], v[48:51], v[32:35]\n"
"  ds_read_b128 v[60:63], v2 offset:27648\n"
"  v_mul_f32 v64, v64, v16\n"
"  v_mul_f32 v65, v65, v17\n"
"  v_mfma_f32_16x16x32_bf16 v[36:39], a[68:71], v[48:51], v[36:39]\n"
"  v_mul_f32 v66, v66, v18\n"
"  v_mul_f32 v67, v67, v19\n"
"  v_mfma_f32_16x16x32_bf16 v[40:43], a[192:195], v[48:51], v[40:43]\n"
"  v_mul_f32 v68, v68, v20\n"
"  v_mul_f32 v69, v69, v21\n"
"  v_mfma_f32_16x16x32_bf16 v[44:47], a[196:199], v[48:51], v[44:47]\n"
"  v_mul_f32 v70, v70, v22\n"
"  v_mul_f32 v71, v71, v23\n"
"  s_waitcnt lgkmcnt(2)\n"
"  v_mfma_f32_16x16x32_bf16 v[32:35], a[72:75], v[52:55], v[32:35]\n"
"  ds_read_b128 v[48:51], v2 offset:28672\n"
"  v_mul_f32 v64, v64, v24\n"
"  v_mul_f32 v65, v65, v25\n"
"  v_mfma_f32_16x16x32_bf16 v[36:39], a[76:79], v[52:55], v[36:39]\n"
"  v_mul_f32 v66, v66, v26\n"
"  v_mul_f32 v67, v67, v27\n"
"  v_mfma_f32_16x16x32_bf16 v[40:43], a[200:203], v[52:55], v[40:43]\n"
"  v_mul_f32 v68, v68, v28\n"
"  v_mul_f32 v69, v69, v29\n"
"  v_mfma_f32_16x16x32_bf16 v[44:47], a[204:207], v[52:55], v[44:47]\n"
"  v_mul_f32 v70, v70, v30\n"
"  v_mul_f32 v71, v71, v31\n"
"  s_waitcnt lgkmcnt(2)\n"
"  v_mfma_f32_16x16x32_bf16 v[32:35], a[80:83], v[56:59], v[32:35]\n"
"  ds_read_b128 v[52:55], v2 offset:29696\n"
"  v_cvt_pk_bf16_f32 v80, v64, v65\n"
"  v_cvt_pk_bf16_f32 v81, v66, v67\n"
"  v_mfma_f32_16x16x32_bf16 v[36:39], a[84:87], v[56:59], v[36:39]\n"
"  v_cvt_pk_bf16_f32 v82, v68, v69\n"
"  v_cvt_pk_bf16_f32 v83, v70, v71\n"
"  v_mfma_f32_16x16x32_bf16 v[40:43], a[208:211], v[56:59], v[40:43]\n"
"  buffer_store_dwordx4 v[80:83], v0, s[40:43], s56 offen sc1\n"
"  v_mfma_f32_16x16x32_bf16 v[44:47], a[212:215], v[56:59], v[44:47]\n"
"  s_waitcnt lgkmcnt(2)\n"
"  v_mfma_f32_16x16x32_bf16 v[32:35], a[88:91], v[60:63], v[32:35]\n"
"  ds_read_b128 v[56:59], v2 offset:30720\n"
"  v_mfma_f32_16x16x32_bf16 v[36:39], a[92:95], v[60:63], v[36:39]\n"
"  v_mfma_f32_16x16x32_bf16 v[40:43], a[216:219], v[60:63], v[40:43]\n"
"  v_mfma_f32_16x16x32_bf16 v[44:47], a[220:223], v[60:63], v[44:47]\n"
"  s_waitcnt lgkmcnt(2)\n"
"  v_mfma_f32_16x16x32_bf16 v[32:35], a[96:99], v[48:51], v[32:35]\n"
"  ds_read_b128 v[60:63], v2 offset:31744\n"
"  v_mfma_f32_16x16x32_bf16 v[36:39], a[100:103], v[48:51], v[36:39]\n"
"  v_mfma_f32_16x16x32_bf16 v[40:43], a[224:227], v[48:51], v[40:43]\n"
"  v_mfma_f32_16x16x32_bf16 v[44:47], a[228:231], v[48:51], v[44:47]\n"
"  s_waitcnt lgkmcnt(2)\n"
"  v_mfma_f32_16x16x32_bf16 v[32:35], a[104:107], v[52:55], v[32:35]\n"
"  v_mfma_f32_16x16x32_bf16 v[36:39], a[108:111], v[52:55], v[36:39]\n"
"  v_mfma_f32_16x16x32_bf16 v[40:43], a[232:235], v[52:55], v[40:43]\n"
"  v_mfma_f32_16x16x32_bf16 v[44:47], a[236:239], v[52:55], v[44:47]\n"
"  s_waitcnt lgkmcnt(1)\n"
"  v_mfma_f32_16x16x32_bf16 v[32:35], a[112:115], v[56:59], v[32:35]\n"
"  v_mfma_f32_16x16x32_bf16 v[36:39], a[116:119], v[56:59], v[36:39]\n"
"  v_mfma_f32_16x16x32_bf16 v[40:43], a[240:243], v[56:59], v[40:43]\n"
"  v_mfma_f32_16x16x32_bf16 v[44:47], a[244:247], v[56:59], v[44:47]\n"
"  s_waitcnt lgkmcnt(0)\n"
"  v_mfma_f32_16x16x32_bf16 v[32:35], a[120:123], v[60:63], v[32:35]\n"
"  v_mfma_f32_16x16x32_bf16 v[36:39], a[124:127], v[60:63], v[36:39]\n"
"  v_mfma_f32_16x16x32_bf16 v[40:43], a[248:251], v[60:63], v[40:43]\n"
"  v_mfma_f32_16x16x32_bf16 v[44:47], a[252:255], v[60:63], v[44:47]\n"
"  s_waitcnt vmcnt(0)\n"
"  s_barrier\n"
"  s_add_u32 s57, s56, 57344\n"
"  s_add_u32 s56, s56, 114688\n"
"  v_xor_b32 v2, 0x8000, v2\n"
"  s_add_u32 s59, s59, 1\n"
"  s_cmp_lt_u32 s59, 8\n"
"  s_cbranch_scc1 L_p2%=\n"
"  s_nop 7\n"
"  v_mul_f32 v64, v32, v32\n"
"  v_mul_f32 v65, v33, v33\n"
"  v_mul_f32 v66, v34, v34\n"
"  v_mul_f32 v67, v35, v35\n"
"  v_mul_f32 v68, v36, v36\n"
"  v_mul_f32 v69, v37, v37\n"
"  v_mul_f32 v70, v38, v38\n"
"  v_mul_f32 v71, v39, v39\n"
"  v_fma_f32 v64, v64, s61, v3\n"
"  v_fma_f32 v65, v65, s61, v3\n"
"  v_fma_f32 v66, v66, s61, v3\n"
"  v_fma_f32 v67, v67, s61, v3\n"
"  v_fma_f32 v68, v68, s61, v3\n"
"  v_fma_f32 v69, v69, s61, v3\n"
"  v_fma_f32 v70, v70, s61, v3\n"
"  v_fma_f32 v71, v71, s61, v3\n"
"  v_mul_f32 v64, v64, v32\n"
"  v_mul_f32 v65, v65, v33\n"
"  v_mul_f32 v66, v66, v34\n"
"  v_mul_f32 v67, v67, v35\n"
"  v_mul_f32 v68, v68, v36\n"
"  v_mul_f32 v69, v69, v37\n"
"  v_mul_f32 v70, v70, v38\n"
"  v_mul_f32 v71, v71, v39\n"
"  v_exp_f32 v64, v64\n"
"  v_exp_f32 v65, v65\n"
"  v_exp_f32 v66, v66\n"
"  v_exp_f32 v67, v67\n"
"  v_exp_f32 v68, v68\n"
"  v_exp_f32 v69, v69\n"
"  v_exp_f32 v70, v70\n"
"  v_exp_f32 v71, v71\n"
"  v_add_f32 v64, 1.0, v64\n"
"  v_add_f32 v65, 1.0, v65\n"
"  v_add_f32 v66, 1.0, v66\n"
"  v_add_f32 v67, 1.0, v67\n"
"  v_add_f32 v68, 1.0, v68\n"
"  v_add_f32 v69, 1.0, v69\n"
"  v_add_f32 v70, 1.0, v70\n"
"  v_add_f32 v71, 1.0, v71\n"
"  v_rcp_f32 v64, v64\n"
"  v_rcp_f32 v65, v65\n"
"  v_rcp_f32 v66, v66\n"
"  v_rcp_f32 v67, v67\n"
"  v_rcp_f32 v68, v68\n"
"  v_rcp_f32 v69, v69\n"
"  v_rcp_f32 v70, v70\n"
"  v_rcp_f32 v71, v71\n"
"  v_mul_f32 v64, v64, v32\n"
"  v_mul_f32 v65, v65, v33\n"
"  v_mul_f32 v66, v66, v34\n"
"  v_mul_f32 v67, v67, v35\n"
"  v_mul_f32 v68, v68, v36\n"
"  v_mul_f32 v69, v69, v37\n"
"  v_mul_f32 v70, v70, v38\n"
"  v_mul_f32 v71, v71, v39\n"
"  v_mul_f32 v64, v64, v40\n"
"  v_mul_f32 v65, v65, v41\n"
"  v_mul_f32 v66, v66, v42\n"
"  v_mul_f32 v67, v67, v43\n"
"  v_mul_f32 v68, v68, v44\n"
"  v_mul_f32 v69, v69, v45\n"
"  v_mul_f32 v70, v70, v46\n"
"  v_mul_f32 v71, v71, v47\n"
"  v_cvt_pk_bf16_f32 v80, v64, v65\n"
"  v_cvt_pk_bf16_f32 v81, v66, v67\n"
"  v_cvt_pk_bf16_f32 v82, v68, v69\n"
"  v_cvt_pk_bf16_f32 v83, v70, v71\n"
"  buffer_store_dwordx4 v[80:83], v0, s[40:43], s57 offen sc1\n"
"  s_waitcnt vmcnt(0)\n"
"  s_barrier\n"
"  s_lshl_b32 s48, s53, 7\n"
"  s_add_u32 s48, s48, 2154496\n"
"  s_cmp_eq_u32 s50, 0\n"
"  s_cselect_b32 s48, s48, 0x7f000000\n"
"  v_mov_b32 v4, 0\n"
"  v_mov_b32 v5, 1\n"
"  s_mov_b64 exec, 1\n"
"  buffer_atomic_add v5, v4, s[40:43], s48 offen sc1\n"
"  s_mov_b64 exec, -1\n"
                :: "s"(ap->ws), "s"(ap->Wg), "s"(ap->Wu), "s"(cg), "s"(rg), "s"(w)
                : BULK_CLOB);
        }
        ARGS(); TS(2);
        asm volatile(
"  s_mov_b64 s[40:41], %0\n"
"  s_mov_b32 s42, 0x0f000000\n"
"  s_mov_b32 s43, 0x00020000\n"
"  s_mov_b32 s47, 0x00020000\n"
"  s_mov_b32 s50, %7\n"
"  v_mbcnt_lo_u32_b32 v1, -1, 0\n"
"  v_mbcnt_hi_u32_b32 v1, -1, v1\n"
"  v_lshlrev_b32 v0, 4, v1\n"
"  s_lshl_b32 s51, s50, 15\n"
"  s_add_u32 s51, s51, 32768\n"
"  s_mov_b32 s52, %5\n"
"  s_mov_b32 s53, %6\n"
"  s_mov_b64 s[64:65], %3\n"
"  s_mov_b32 s66, 2097152\n"
"  s_mov_b32 s67, 0x00020000\n"
"  s_mov_b64 s[68:69], %4\n"
"  s_mov_b32 s70, 2097152\n"
"  s_mov_b32 s71, 0x00020000\n"
"  s_mov_b32 s60, 0\n"
"  v_lshrrev_b32 v2, 2, v1\n"
"  v_mul_u32_u24 v2, 1024, v2\n"
"  v_and_b32 v3, 0x30, v0\n"
"  v_add_u32 v2, v2, v3\n"
"  v_lshrrev_b32 v4, 4, v1\n"
"  v_mul_u32_u24 v4, 256, v4\n"
"  v_bfe_u32 v5, v1, 2, 2\n"
"  v_lshl_add_u32 v4, v5, 6, v4\n"
"  v_and_b32 v5, 3, v1\n"
"  v_lshl_add_u32 v4, v5, 3, v4\n"
"  v_add_u32 v4, s51, v4\n"
"  s_lshl_b32 s48, s52, 6\n"
"  s_lshl_b32 s54, s50, 20\n"
"  s_add_u32 s54, s54, s48\n"
"  s_mul_i32 s55, s50, 458752\n"
"  s_add_u32 s55, s55, s48\n"
"  s_mov_b64 s[44:45], %2\n"
"  s_mov_b32 s46, 1835008\n"
"  v_add_u32 v113, 15360, v2\n"
"  v_add_u32 v114, 30720, v2\n"
"  v_add_u32 v115, 46080, v2\n"
"  s_add_u32 m0, s51, 0\n"
"  s_add_u32 s48, s55, 0\n"
"  buffer_load_dwordx4 v2, s[44:47], s48 offen lds\n"
"  buffer_load_dwordx4 v113, s[44:47], s48 offen offset:1024 lds\n"
"  buffer_load_dwordx4 v114, s[44:47], s48 offen offset:2048 lds\n"
"  buffer_load_dwordx4 v115, s[44:47], s48 offen offset:3072 lds\n"
"  s_add_u32 m0, s51, 4096\n"
"  s_add_u32 s48, s55, 65536\n"
"  buffer_load_dwordx4 v2, s[44:47], s48 offen lds\n"
"  buffer_load_dwordx4 v113, s[44:47], s48 offen offset:1024 lds\n"
"  buffer_load_dwordx4 v114, s[44:47], s48 offen offset:2048 lds\n"
"  buffer_load_dwordx4 v115, s[44:47], s48 offen offset:3072 lds\n"
"  s_add_u32 m0, s51, 8192\n"
"  s_add_u32 s48, s55, 131072\n"
"  buffer_load_dwordx4 v2, s[44:47], s48 offen lds\n"
"  buffer_load_dwordx4 v113, s[44:47], s48 offen offset:1024 lds\n"
"  buffer_load_dwordx4 v114, s[44:47], s48 offen offset:2048 lds\n"
"  buffer_load_dwordx4 v115, s[44:47], s48 offen offset:3072 lds\n"
"  s_add_u32 m0, s51, 12288\n"
"  s_add_u32 s48, s55, 196608\n"
"  buffer_load_dwordx4 v2, s[44:47], s48 offen lds\n"
"  buffer_load_dwordx4 v113, s[44:47], s48 offen offset:1024 lds\n"
"  buffer_load_dwordx4 v114, s[44:47], s48 offen offset:2048 lds\n"
"  buffer_load_dwordx4 v115, s[44:47], s48 offen offset:3072 lds\n"
"  s_add_u32 m0, s51, 16384\n"
"  s_add_u32 s48, s55, 262144\n"
"  buffer_load_dwordx4 v2, s[44:47], s48 offen lds\n"
"  buffer_load_dwordx4 v113, s[44:47], s48 offen offset:1024 lds\n"
"  buffer_load_dwordx4 v114, s[44:47], s48 offen offset:2048 lds\n"
"  buffer_load_dwordx4 v115, s[44:47], s48 offen offset:3072 lds\n"
"  s_add_u32 m0, s51, 20480\n"
"  s_add_u32 s48, s55, 327680\n"
"  buffer_load_dwordx4 v2, s[44:47], s48 offen lds\n"
"  buffer_load_dwordx4 v113, s[44:47], s48 offen offset:1024 lds\n"
"  buffer_load_dwordx4 v114, s[44:47], s48 offen offset:2048 lds\n"
"  buffer_load_dwordx4 v115, s[44:47], s48 offen offset:3072 lds\n"
"  s_add_u32 m0, s51, 24576\n"
"  s_add_u32 s48, s55, 393216\n"
"  buffer_load_dwordx4 v2, s[44:47], s48 offen lds\n"
"  buffer_load_dwordx4 v113, s[44:47], s48 offen offset:1024 lds\n"
"  buffer_load_dwordx4 v114, s[44:47], s48 offen offset:2048 lds\n"
"  buffer_load_dwordx4 v115, s[44:47], s48 offen offset:3072 lds\n"
"  s_mov_b64 s[44:45], %1\n"
"  s_mov_b32 s46, 4194304\n"
"  s_add_u32 m0, s51, 28672\n"
"  s_add_u32 s48, s54, 0\n"
"  buffer_load_dwordx4 v2, s[44:47], s48 offen lds\n"
"  buffer_load_dwordx4 v113, s[44:47], s48 offen offset:1024 lds\n"
"  buffer_load_dwordx4 v114, s[44:47], s48 offen offset:2048 lds\n"
"  buffer_load_dwordx4 v115, s[44:47], s48 offen offset:3072 lds\n"
"  s_waitcnt vmcnt(28)\n"
"  ds_read_b64_tr_b16 a[0:1], v4 offset:0\n"
"  ds_read_b64_tr_b16 a[2:3], v4 offset:1024\n"
"  ds_read_b64_tr_b16 a[4:5], v4 offset:32\n"
"  ds_read_b64_tr_b16 a[6:7], v4 offset:1056\n"
"  ds_read_b64_tr_b16 a[8:9], v4 offset:2048\n"
"  ds_read_b64_tr_b16 a[10:11], v4 offset:3072\n"
"  ds_read_b64_tr_b16 a[12:13], v4 offset:2080\n"
"  ds_read_b64_tr_b16 a[14:15], v4 offset:3104\n"
"  s_waitcnt vmcnt(24)\n"
"  ds_read_b64_tr_b16 a[16:17], v4 offset:4096\n"
"  ds_read_b64_tr_b16 a[18:19], v4 offset:5120\n"
"  ds_read_b64_tr_b16 a[20:21], v4 offset:4128\n"
"  ds_read_b64_tr_b16 a[22:23], v4 offset:5152\n"
"  s_waitcnt lgkmcnt(4)\n"
"  s_add_u32 m0, s51, 0\n"
"  s_add_u32 s48, s54, 65536\n"
"  buffer_load_dwordx4 v2, s[44:47], s48 offen lds\n"
"  buffer_load_dwordx4 v113, s[44:47], s48 offen offset:1024 lds\n"
"  buffer_load_dwordx4 v114, s[44:47], s48 offen offset:2048 lds\n"
"  buffer_load_dwordx4 v115, s[44:47], s48 offen offset:3072 lds\n"
"  ds_read_b64_tr_b16 a[24:25], v4 offset:6144\n"
"  ds_read_b64_tr_b16 a[26:27], v4 offset:7168\n"
"  ds_read_b64_tr_b16 a[28:29], v4 offset:6176\n"
"  ds_read_b64_tr_b16 a[30:31], v4 offset:7200\n"
"  s_waitcnt vmcnt(24)\n"
"  ds_read_b64_tr_b16 a[32:33], v4 offset:8192\n"
"  ds_read_b64_tr_b16 a[34:35], v4 offset:9216\n"
"  ds_read_b64_tr_b16 a[36:37], v4 offset:8224\n"
"  ds_read_b64_tr_b16 a[38:39], v4 offset:9248\n"
"  s_waitcnt lgkmcnt(4)\n"
"  s_add_u32 m0, s51, 4096\n"
"  s_add_u32 s48, s54, 131072\n"
"  buffer_load_dwordx4 v2, s[44:47], s48 offen lds\n"
"  buffer_load_dwordx4 v113, s[44:47], s48 offen offset:1024 lds\n"
"  buffer_load_dwordx4 v114, s[44:47], s48 offen offset:2048 lds\n"
"  buffer_load_dwordx4 v115, s[44:47], s48 offen offset:3072 lds\n"
"  ds_read_b64_tr_b16 a[40:41], v4 offset:10240\n"
"  ds_read_b64_tr_b16 a[42:43], v4 offset:11264\n"
"  ds_read_b64_tr_b16 a[44:45], v4 offset:10272\n"
"  ds_read_b64_tr_b16 a[46:47], v4 offset:11296\n"
"  s_waitcnt vmcnt(24)\n"
"  ds_read_b64_tr_b16 a[48:49], v4 offset:12288\n"
"  ds_read_b64_tr_b16 a[50:51], v4 offset:13312\n"
"  ds_read_b64_tr_b16 a[52:53], v4 offset:12320\n"
"  ds_read_b64_tr_b16 a[54:55], v4 offset:13344\n"
"  s_waitcnt lgkmcnt(4)\n"
"  s_add_u32 m0, s51, 8192\n"
"  s_add_u32 s48, s54, 196608\n"
"  buffer_load_dwordx4 v2, s[44:47], s48 offen lds\n"
"  buffer_load_dwordx4 v113, s[44:47], s48 offen offset:1024 lds\n"
"  buffer_load_dwordx4 v114, s[44:47], s48 offen offset:2048 lds\n"
"  buffer_load_dwordx4 v115, s[44:47], s48 offen offset:3072 lds\n"
"  ds_read_b64_tr_b16 a[56:57], v4 offset:14336\n"
"  ds_read_b64_tr_b16 a[58:59], v4 offset:15360\n"
"  ds_read_b64_tr_b16 a[60:61], v4 offset:14368\n"
"  ds_read_b64_tr_b16 a[62:63], v4 offset:15392\n"
"  s_waitcnt vmcnt(24)\n"
"  ds_read_b64_tr_b16 a[64:65], v4 offset:16384\n"
"  ds_read_b64_tr_b16 a[66:67], v4 offset:17408\n"
"  ds_read_b64_tr_b16 a[68:69], v4 offset:16416\n"
"  ds_read_b64_tr_b16 a[70:71], v4 offset:17440\n"
"  s_waitcnt lgkmcnt(4)\n"
"  s_add_u32 m0, s51, 12288\n"
"  s_add_u32 s48, s54, 262144\n"
"  buffer_load_dwordx4 v2, s[44:47], s48 offen lds\n"
"  buffer_load_dwordx4 v113, s[44:47], s48 offen offset:1024 lds\n"
"  buffer_load_dwordx4 v114, s[44:47], s48 offen offset:2048 lds\n"
"  buffer_load_dwordx4 v115, s[44:47], s48 offen offset:3072 lds\n"
"  ds_read_b64_tr_b16 a[72:73], v4 offset:18432\n"
"  ds_read_b64_tr_b16 a[74:75], v4 offset:19456\n"
"  ds_read_b64_tr_b16 a[76:77], v4 offset:18464\n"
"  ds_read_b64_tr_b16 a[78:79], v4 offset:19488\n"
"  s_waitcnt vmcnt(24)\n"
"  ds_read_b64_tr_b16 a[80:81], v4 offset:20480\n"
"  ds_read_b64_tr_b16 a[82:83], v4 offset:21504\n"
"  ds_read_b64_tr_b16 a[84:85], v4 offset:20512\n"
"  ds_read_b64_tr_b16 a[86:87], v4 offset:21536\n"
"  s_waitcnt lgkmcnt(4)\n"
"  s_add_u32 m0, s51, 16384\n"
"  s_add_u32 s48, s54, 327680\n"
"  buffer_load_dwordx4 v2, s[44:47], s48 offen lds\n"
"  buffer_load_dwordx4 v113, s[44:47], s48 offen offset:1024 lds\n"
"  buffer_load_dwordx4 v114, s[44:47], s48 offen offset:2048 lds\n"
"  buffer_load_dwordx4 v115, s[44:47], s48 offen offset:3072 lds\n"
"  ds_read_b64_tr_b16 a[88:89], v4 offset:22528\n"
"  ds_read_b64_tr_b16 a[90:91], v4 offset:23552\n"
"  ds_read_b64_tr_b16 a[92:93], v4 offset:22560\n"
"  ds_read_b64_tr_b16 a[94:95], v4 offset:23584\n"
"  s_waitcnt vmcnt(24)\n"
"  ds_read_b64_tr_b16 a[96:97], v4 offset:24576\n"
"  ds_read_b64_tr_b16 a[98:99], v4 offset:25600\n"
"  ds_read_b64_tr_b16 a[100:101], v4 offset:24608\n"
"  ds_read_b64_tr_b16 a[102:103], v4 offset:25632\n"
"  s_waitcnt lgkmcnt(4)\n"
"  s_add_u32 m0, s51, 20480\n"
"  s_add_u32 s48, s54, 393216\n"
"  buffer_load_dwordx4 v2, s[44:47], s48 offen lds\n"
"  buffer_load_dwordx4 v113, s[44:47], s48 offen offset:1024 lds\n"
"  buffer_load_dwordx4 v114, s[44:47], s48 offen offset:2048 lds\n"
"  buffer_load_dwordx4 v115, s[44:47], s48 offen offset:3072 lds\n"
"  ds_read_b64_tr_b16 a[104:105], v4 offset:26624\n"
"  ds_read_b64_tr_b16 a[106:107], v4 offset:27648\n"
"  ds_read_b64_tr_b16 a[108:109], v4 offset:26656\n"
"  ds_read_b64_tr_b16 a[110:111], v4 offset:27680\n"
"  s_waitcnt vmcnt(24)\n"
"  ds_read_b64_tr_b16 a[112:113], v4 offset:28672\n"
"  ds_read_b64_tr_b16 a[114:115], v4 offset:29696\n"
"  ds_read_b64_tr_b16 a[116:117], v4 offset:28704\n"
"  ds_read_b64_tr_b16 a[118:119], v4 offset:29728\n"
"  s_waitcnt lgkmcnt(4)\n"
"  s_add_u32 m0, s51, 24576\n"
"  s_add_u32 s48, s54, 458752\n"
"  buffer_load_dwordx4 v2, s[44:47], s48 offen lds\n"
"  buffer_load_dwordx4 v113, s[44:47], s48 offen offset:1024 lds\n"
"  buffer_load_dwordx4 v114, s[44:47], s48 offen offset:2048 lds\n"
"  buffer_load_dwordx4 v115, s[44:47], s48 offen offset:3072 lds\n"
"  ds_read_b64_tr_b16 a[120:121], v4 offset:30720\n"
"  ds_read_b64_tr_b16 a[122:123], v4 offset:31744\n"
"  ds_read_b64_tr_b16 a[124:125], v4 offset:30752\n"
"  ds_read_b64_tr_b16 a[126:127], v4 offset:31776\n"
"  s_waitcnt vmcnt(24)\n"
"  ds_read_b64_tr_b16 a[128:129], v4 offset:0\n"
"  ds_read_b64_tr_b16 a[130:131], v4 offset:1024\n"
"  ds_read_b64_tr_b16 a[132:133], v4 offset:32\n"
"  ds_read_b64_tr_b16 a[134:135], v4 offset:1056\n"
"  s_waitcnt lgkmcnt(4)\n"
"  s_add_u32 m0, s51, 28672\n"
"  s_add_u32 s48, s54, 524288\n"
"  buffer_load_dwordx4 v2, s[44:47], s48 offen lds\n"
"  buffer_load_dwordx4 v113, s[44:47], s48 offen offset:1024 lds\n"
"  buffer_load_dwordx4 v114, s[44:47], s48 offen offset:2048 lds\n"
"  buffer_load_dwordx4 v115, s[44:47], s48 offen offset:3072 lds\n"
"  ds_read_b64_tr_b16 a[136:137], v4 offset:2048\n"
"  ds_read_b64_tr_b16 a[138:139], v4 offset:3072\n"
"  ds_read_b64_tr_b16 a[140:141], v4 offset:2080\n"
"  ds_read_b64_tr_b16 a[142:143], v4 offset:3104\n"
"  s_waitcnt vmcnt(24)\n"
"  ds_read_b64_tr_b16 a[144:145], v4 offset:4096\n"
"  ds_read_b64_tr_b16 a[146:147], v4 offset:5120\n"
"  ds_read_b64_tr_b16 a[148:149], v4 offset:4128\n"
"  ds_read_b64_tr_b16 a[150:151], v4 offset:5152\n"
"  s_waitcnt lgkmcnt(4)\n"
"  s_add_u32 m0, s51, 0\n"
"  s_add_u32 s48, s54, 589824\n"
"  buffer_load_dwordx4 v2, s[44:47], s48 offen lds\n"
"  buffer_load_dwordx4 v113, s[44:47], s48 offen offset:1024 lds\n"
"  buffer_load_dwordx4 v114, s[44:47], s48 offen offset:2048 lds\n"
"  buffer_load_dwordx4 v115, s[44:47], s48 offen offset:3072 lds\n"
"  ds_read_b64_tr_b16 a[152:153], v4 offset:6144\n"
"  ds_read_b64_tr_b16 a[154:155], v4 offset:7168\n"
"  ds_read_b64_tr_b16 a[156:157], v4 offset:6176\n"
"  ds_read_b64_tr_b16 a[158:159], v4 offset:7200\n"
"  s_waitcnt vmcnt(24)\n"
"  ds_read_b64_tr_b16 a[160:161], v4 offset:8192\n"
"  ds_read_b64_tr_b16 a[162:163], v4 offset:9216\n"
"  ds_read_b64_tr_b16 a[164:165], v4 offset:8224\n"
"  ds_read_b64_tr_b16 a[166:167], v4 offset:9248\n"
"  s_waitcnt lgkmcnt(4)\n"
"  s_add_u32 m0, s51, 4096\n"
"  s_add_u32 s48, s54, 655360\n"
"  buffer_load_dwordx4 v2, s[44:47], s48 offen lds\n"
"  buffer_load_dwordx4 v113, s[44:47], s48 offen offset:1024 lds\n"
"  buffer_load_dwordx4 v114, s[44:47], s48 offen offset:2048 lds\n"
"  buffer_load_dwordx4 v115, s[44:47], s48 offen offset:3072 lds\n"
"  ds_read_b64_tr_b16 a[168:169], v4 offset:10240\n"
"  ds_read_b64_tr_b16 a[170:171], v4 offset:11264\n"
"  ds_read_b64_tr_b16 a[172:173], v4 offset:10272\n"
"  ds_read_b64_tr_b16 a[174:175], v4 offset:11296\n"
"  s_waitcnt vmcnt(24)\n"
"  ds_read_b64_tr_b16 a[176:177], v4 offset:12288\n"
"  ds_read_b64_tr_b16 a[178:179], v4 offset:13312\n"
"  ds_read_b64_tr_b16 a[180:181], v4 offset:12320\n"
"  ds_read_b64_tr_b16 a[182:183], v4 offset:13344\n"
"  s_waitcnt lgkmcnt(4)\n"
"  s_add_u32 m0, s51, 8192\n"
"  s_add_u32 s48, s54, 720896\n"
"  buffer_load_dwordx4 v2, s[44:47], s48 offen lds\n"
"  buffer_load_dwordx4 v113, s[44:47], s48 offen offset:1024 lds\n"
"  buffer_load_dwordx4 v114, s[44:47], s48 offen offset:2048 lds\n"
"  buffer_load_dwordx4 v115, s[44:47], s48 offen offset:3072 lds\n"
"  ds_read_b64_tr_b16 a[184:185], v4 offset:14336\n"
"  ds_read_b64_tr_b16 a[186:187], v4 offset:15360\n"
"  ds_read_b64_tr_b16 a[188:189], v4 offset:14368\n"
"  ds_read_b64_tr_b16 a[190:191], v4 offset:15392\n"
"  s_waitcnt vmcnt(24)\n"
"  ds_read_b64_tr_b16 a[192:193], v4 offset:16384\n"
"  ds_read_b64_tr_b16 a[194:195], v4 offset:17408\n"
"  ds_read_b64_tr_b16 a[196:197], v4 offset:16416\n"
"  ds_read_b64_tr_b16 a[198:199], v4 offset:17440\n"
"  s_waitcnt lgkmcnt(4)\n"
"  s_add_u32 m0, s51, 12288\n"
"  s_add_u32 s48, s54, 786432\n"
"  buffer_load_dwordx4 v2, s[44:47], s48 offen lds\n"
"  buffer_load_dwordx4 v113, s[44:47], s48 offen offset:1024 lds\n"
"  buffer_load_dwordx4 v114, s[44:47], s48 offen offset:2048 lds\n"
"  buffer_load_dwordx4 v115, s[44:47], s48 offen offset:3072 lds\n"
"  ds_read_b64_tr_b16 a[200:201], v4 offset:18432\n"
"  ds_read_b64_tr_b16 a[202:203], v4 offset:19456\n"
"  ds_read_b64_tr_b16 a[204:205], v4 offset:18464\n"
"  ds_read_b64_tr_b16 a[206:207], v4 offset:19488\n"
"  s_waitcnt vmcnt(24)\n"
"  ds_read_b64_tr_b16 a[208:209], v4 offset:20480\n"
"  ds_read_b64_tr_b16 a[210:211], v4 offset:21504\n"
"  ds_read_b64_tr_b16 a[212:213], v4 offset:20512\n"
"  ds_read_b64_tr_b16 a[214:215], v4 offset:21536\n"
"  s_waitcnt lgkmcnt(4)\n"
"  s_add_u32 m0, s51, 16384\n"
"  s_add_u32 s48, s54, 851968\n"
"  buffer_load_dwordx4 v2, s[44:47], s48 offen lds\n"
"  buffer_load_dwordx4 v113, s[44:47], s48 offen offset:1024 lds\n"
"  buffer_load_dwordx4 v114, s[44:47], s48 offen offset:2048 lds\n"
"  buffer_load_dwordx4 v115, s[44:47], s48 offen offset:3072 lds\n"
"  ds_read_b64_tr_b16 a[216:217], v4 offset:22528\n"
"  ds_read_b64_tr_b16 a[218:219], v4 offset:23552\n"
"  ds_read_b64_tr_b16 a[220:221], v4 offset:22560\n"
"  ds_read_b64_tr_b16 a[222:223], v4 offset:23584\n"
"  s_waitcnt vmcnt(24)\n"
"  ds_read_b64_tr_b16 a[224:225], v4 offset:24576\n"
"  ds_read_b64_tr_b16 a[226:227], v4 offset:25600\n"
"  ds_read_b64_tr_b16 a[228:229], v4 offset:24608\n"
"  ds_read_b64_tr_b16 a[230:231], v4 offset:25632\n"
"  s_waitcnt lgkmcnt(4)\n"
"  s_add_u32 m0, s51, 20480\n"
"  s_add_u32 s48, s54, 917504\n"
"  buffer_load_dwordx4 v2, s[44:47], s48 offen lds\n"
"  buffer_load_dwordx4 v113, s[44:47], s48 offen offset:1024 lds\n"
"  buffer_load_dwordx4 v114, s[44:47], s48 offen offset:2048 lds\n"
"  buffer_load_dwordx4 v115, s[44:47], s48 offen offset:3072 lds\n"
"  ds_read_b64_tr_b16 a[232:233], v4 offset:26624\n"
"  ds_read_b64_tr_b16 a[234:235], v4 offset:27648\n"
"  ds_read_b64_tr_b16 a[236:237], v4 offset:26656\n"
"  ds_read_b64_tr_b16 a[238:239], v4 offset:27680\n"
"  s_waitcnt vmcnt(24)\n"
"  ds_read_b64_tr_b16 a[240:241], v4 offset:28672\n"
"  ds_read_b64_tr_b16 a[242:243], v4 offset:29696\n"
"  ds_read_b64_tr_b16 a[244:245], v4 offset:28704\n"
"  ds_read_b64_tr_b16 a[246:247], v4 offset:29728\n"
"  s_waitcnt lgkmcnt(4)\n"
"  s_add_u32 m0, s51, 24576\n"
"  s_add_u32 s48, s54, 983040\n"
"  buffer_load_dwordx4 v2, s[44:47], s48 offen lds\n"
"  buffer_load_dwordx4 v113, s[44:47], s48 offen offset:1024 lds\n"
"  buffer_load_dwordx4 v114, s[44:47], s48 offen offset:2048 lds\n"
"  buffer_load_dwordx4 v115, s[44:47], s48 offen offset:3072 lds\n"
"  ds_read_b64_tr_b16 a[248:249], v4 offset:30720\n"
"  ds_read_b64_tr_b16 a[250:251], v4 offset:31744\n"
"  ds_read_b64_tr_b16 a[252:253], v4 offset:30752\n"
"  ds_read_b64_tr_b16 a[254:255], v4 offset:31776\n"
"  s_waitcnt vmcnt(24)\n"
"  ds_read_b64_tr_b16 v[144:145], v4 offset:0\n"
"  ds_read_b64_tr_b16 v[146:147], v4 offset:1024\n"
"  ds_read_b64_tr_b16 v[148:149], v4 offset:32\n"
"  ds_read_b64_tr_b16 v[150:151], v4 offset:1056\n"
"  s_waitcnt lgkmcnt(4)\n"
"  ds_read_b64_tr_b16 v[152:153], v4 offset:2048\n"
"  ds_read_b64_tr_b16 v[154:155], v4 offset:3072\n"
"  ds_read_b64_tr_b16 v[156:157], v4 offset:2080\n"
"  ds_read_b64_tr_b16 v[158:159], v4 offset:3104\n"
"  s_waitcnt vmcnt(20)\n"
"  ds_read_b64_tr_b16 v[160:161], v4 offset:4096\n"
"  ds_read_b64_tr_b16 v[162:163], v4 offset:5120\n"
"  ds_read_b64_tr_b16 v[164:165], v4 offset:4128\n"
"  ds_read_b64_tr_b16 v[166:167], v4 offset:5152\n"
"  s_waitcnt lgkmcnt(4)\n"
"  ds_read_b64_tr_b16 v[168:169], v4 offset:6144\n"
"  ds_read_b64_tr_b16 v[170:171], v4 offset:7168\n"
"  ds_read_b64_tr_b16 v[172:173], v4 offset:6176\n"
"  ds_read_b64_tr_b16 v[174:175], v4 offset:7200\n"
"  s_waitcnt vmcnt(16)\n"
"  ds_read_b64_tr_b16 v[176:177], v4 offset:8192\n"
"  ds_read_b64_tr_b16 v[178:179], v4 offset:9216\n"
"  ds_read_b64_tr_b16 v[180:181], v4 offset:8224\n"
"  ds_read_b64_tr_b16 v[182:183], v4 offset:9248\n"
"  s_waitcnt lgkmcnt(4)\n"
"  ds_read_b64_tr_b16 v[184:185], v4 offset:10240\n"
"  ds_read_b64_tr_b16 v[186:187], v4 offset:11264\n"
"  ds_read_b64_tr_b16 v[188:189], v4 offset:10272\n"
"  ds_read_b64_tr_b16 v[190:191], v4 offset:11296\n"
"  s_waitcnt vmcnt(12)\n"
"  ds_read_b64_tr_b16 v[192:193], v4 offset:12288\n"
"  ds_read_b64_tr_b16 v[194:195], v4 offset:13312\n"
"  ds_read_b64_tr_b16 v[196:197], v4 offset:12320\n"
"  ds_read_b64_tr_b16 v[198:199], v4 offset:13344\n"
"  s_waitcnt lgkmcnt(4)\n"
"  ds_read_b64_tr_b16 v[200:201], v4 offset:14336\n"
"  ds_read_b64_tr_b16 v[202:203], v4 offset:15360\n"
"  ds_read_b64_tr_b16 v[204:205], v4 offset:14368\n"
"  ds_read_b64_tr_b16 v[206:207], v4 offset:15392\n"
"  s_waitcnt vmcnt(8)\n"
"  ds_read_b64_tr_b16 v[208:209], v4 offset:16384\n"
"  ds_read_b64_tr_b16 v[210:211], v4 offset:17408\n"
"  ds_read_b64_tr_b16 v[212:213], v4 offset:16416\n"
"  ds_read_b64_tr_b16 v[214:215], v4 offset:17440\n"
"  s_waitcnt lgkmcnt(4)\n"
"  ds_read_b64_tr_b16 v[216:217], v4 offset:18432\n"
"  ds_read_b64_tr_b16 v[218:219], v4 offset:19456\n"
"  ds_read_b64_tr_b16 v[220:221], v4 offset:18464\n"
"  ds_read_b64_tr_b16 v[222:223], v4 offset:19488\n"
"  s_waitcnt vmcnt(4)\n"
"  ds_read_b64_tr_b16 v[224:225], v4 offset:20480\n"
"  ds_read_b64_tr_b16 v[226:227], v4 offset:21504\n"
"  ds_read_b64_tr_b16 v[228:229], v4 offset:20512\n"
"  ds_read_b64_tr_b16 v[230:231], v4 offset:21536\n"
"  s_waitcnt lgkmcnt(4)\n"
"  ds_read_b64_tr_b16 v[232:233], v4 offset:22528\n"
"  ds_read_b64_tr_b16 v[234:235], v4 offset:23552\n"
"  ds_read_b64_tr_b16 v[236:237], v4 offset:22560\n"
"  ds_read_b64_tr_b16 v[238:239], v4 offset:23584\n"
"  s_waitcnt vmcnt(0)\n"
"  ds_read_b64_tr_b16 v[240:241], v4 offset:24576\n"
"  ds_read_b64_tr_b16 v[242:243], v4 offset:25600\n"
"  ds_read_b64_tr_b16 v[244:245], v4 offset:24608\n"
"  ds_read_b64_tr_b16 v[246:247], v4 offset:25632\n"
"  s_waitcnt lgkmcnt(4)\n"
"  ds_read_b64_tr_b16 v[248:249], v4 offset:26624\n"
"  ds_read_b64_tr_b16 v[250:251], v4 offset:27648\n"
"  ds_read_b64_tr_b16 v[252:253], v4 offset:26656\n"
"  ds_read_b64_tr_b16 v[254:255], v4 offset:27680\n"
"  s_waitcnt lgkmcnt(0)\n"
"  v_mov_b32 v2, 0\n"
"  v_and_b32 v3, 15, v1\n"
"  v_lshlrev_b32 v3, 10, v3\n"
"  v_lshrrev_b32 v7, 4, v1\n"
"  v_lshl_add_u32 v3, v7, 3, v3\n"
"  s_lshl_b32 s48, s50, 5\n"
"  s_cmp_lt_u32 s50, 2\n"
"  s_cselect_b32 s48, s48, 0x7f000000\n"
"  v_add_u32 v3, s48, v3\n"
"  s_lshl_b32 s48, s50, 11\n"
"  v_add_u32 v4, s48, v0\n"
"  s_and_b32 s48, s50, 1\n"
"  s_lshl_b32 s48, s48, 10\n"
"  v_add_u32 v5, s48, v0\n"
"L_p4%=:\n"
"  s_mul_i32 s57, s53, 57344\n"
"  s_mul_i32 s48, s50, 14336\n"
"  s_add_u32 s57, s57, s48\n"
"  s_add_u32 s57, s57, 117440512\n"
"  s_lshl_b32 s56, s53, 17\n"
"  s_lshl_b32 s48, s50, 15\n"
"  s_add_u32 s56, s56, s48\n"
"  s_add_u32 s56, s56, 100663296\n"
"  s_lshl_b32 s58, s53, 14\n"
"  s_lshl_b32 s48, s52, 6\n"
"  s_add_u32 s58, s58, s48\n"
"  s_lshr_b32 s61, s53, 4\n"
"  s_lshl_b32 s61, s61, 7\n"
"  s_add_u32 s61, s61, 2154496\n"
"  s_cmp_lg_u32 s50, 0\n"
"  s_cbranch_scc1 L_wh%=d\n"
"  s_mov_b32 s59, 0\n"
"L_wh%=:\n"
"  buffer_load_dword v6, v2, s[40:43], s61 offen sc1\n"
"  s_waitcnt vmcnt(0)\n"
"  v_readfirstlane_b32 s48, v6\n"
"  s_nop 3\n"
"  s_cmp_ge_u32 s48, 14\n"
"  s_cbranch_scc1 L_wh%=d\n"
"  s_cmp_lg_u32 s60, 0\n"
"  s_cbranch_scc1 L_wh%=d\n"
"  s_sleep 1\n"
"  s_add_u32 s59, s59, 1\n"
"  s_cmp_gt_u32 s59, 200000\n"
"  s_cselect_b32 s60, 1, 0\n"
"  s_branch L_wh%=\n"
"L_wh%=d:\n"
"  s_barrier\n"
"  buffer_load_dwordx2 v[16:17], v3, s[64:67], s58 offen\n"
"  s_add_u32 s49, s57, 0\n"
"  buffer_load_dwordx4 v[32:35], v0, s[40:43], s49 offen offset:0 sc1\n"
"  buffer_load_dwordx4 v[36:39], v0, s[40:43], s49 offen offset:1024 sc1\n"
"  buffer_load_dwordx4 v[40:43], v0, s[40:43], s49 offen offset:2048 sc1\n"
"  buffer_load_dwordx4 v[44:47], v0, s[40:43], s49 offen offset:3072 sc1\n"
"  s_add_u32 s49, s57, 4096\n"
"  buffer_load_dwordx4 v[48:51], v0, s[40:43], s49 offen offset:0 sc1\n"
"  buffer_load_dwordx4 v[52:55], v0, s[40:43], s49 offen offset:1024 sc1\n"
"  buffer_load_dwordx4 v[56:59], v0, s[40:43], s49 offen offset:2048 sc1\n"
"  buffer_load_dwordx4 v[60:63], v0, s[40:43], s49 offen offset:3072 sc1\n"
"  s_add_u32 s49, s57, 8192\n"
"  buffer_load_dwordx4 v[64:67], v0, s[40:43], s49 offen offset:0 sc1\n"
"  buffer_load_dwordx4 v[68:71], v0, s[40:43], s49 offen offset:1024 sc1\n"
"  buffer_load_dwordx4 v[72:75], v0, s[40:43], s49 offen offset:2048 sc1\n"
"  buffer_load_dwordx4 v[76:79], v0, s[40:43], s49 offen offset:3072 sc1\n"
"  s_add_u32 s49, s57, 12288\n"
"  buffer_load_dwordx4 v[80:83], v0, s[40:43], s49 offen offset:0 sc1\n"
"  buffer_load_dwordx4 v[84:87], v0, s[40:43], s49 offen offset:1024 sc1\n"
"  s_waitcnt vmcnt(13)\n"
"  v_mfma_f32_16x16x32_bf16 v[8:11], a[0:3], v[32:35], 0\n"
"  v_mfma_f32_16x16x32_bf16 v[12:15], a[4:7], v[32:35], 0\n"
"  s_waitcnt vmcnt(12)\n"
"  v_mfma_f32_16x16x32_bf16 v[8:11], a[8:11], v[36:39], v[8:11]\n"
"  v_mfma_f32_16x16x32_bf16 v[12:15], a[12:15], v[36:39], v[12:15]\n"
"  s_waitcnt vmcnt(11)\n"
"  v_mfma_f32_16x16x32_bf16 v[8:11], a[16:19], v[40:43], v[8:11]\n"
"  v_mfma_f32_16x16x32_bf16 v[12:15], a[20:23], v[40:43], v[12:15]\n"
"  s_waitcnt vmcnt(10)\n"
"  v_mfma_f32_16x16x32_bf16 v[8:11], a[24:27], v[44:47], v[8:11]\n"
"  v_mfma_f32_16x16x32_bf16 v[12:15], a[28:31], v[44:47], v[12:15]\n"
"  s_waitcnt vmcnt(9)\n"
"  v_mfma_f32_16x16x32_bf16 v[8:11], a[32:35], v[48:51], v[8:11]\n"
"  v_mfma_f32_16x16x32_bf16 v[12:15], a[36:39], v[48:51], v[12:15]\n"
"  s_waitcnt vmcnt(8)\n"
"  v_mfma_f32_16x16x32_bf16 v[8:11], a[40:43], v[52:55], v[8:11]\n"
"  v_mfma_f32_16x16x32_bf16 v[12:15], a[44:47], v[52:55], v[12:15]\n"
"  s_waitcnt vmcnt(7)\n"
"  v_mfma_f32_16x16x32_bf16 v[8:11], a[48:51], v[56:59], v[8:11]\n"
"  v_mfma_f32_16x16x32_bf16 v[12:15], a[52:55], v[56:59], v[12:15]\n"
"  s_waitcnt vmcnt(6)\n"
"  v_mfma_f32_16x16x32_bf16 v[8:11], a[56:59], v[60:63], v[8:11]\n"
"  v_mfma_f32_16x16x32_bf16 v[12:15], a[60:63], v[60:63], v[12:15]\n"
"  s_waitcnt vmcnt(5)\n"
"  v_mfma_f32_16x16x32_bf16 v[8:11], a[64:67], v[64:67], v[8:11]\n"
"  v_mfma_f32_16x16x32_bf16 v[12:15], a[68:71], v[64:67], v[12:15]\n"
"  s_waitcnt vmcnt(4)\n"
"  v_mfma_f32_16x16x32_bf16 v[8:11], a[72:75], v[68:71], v[8:11]\n"
"  v_mfma_f32_16x16x32_bf16 v[12:15], a[76:79], v[68:71], v[12:15]\n"
"  s_waitcnt vmcnt(3)\n"
"  v_mfma_f32_16x16x32_bf16 v[8:11], a[80:83], v[72:75], v[8:11]\n"
"  v_mfma_f32_16x16x32_bf16 v[12:15], a[84:87], v[72:75], v[12:15]\n"
"  s_waitcnt vmcnt(2)\n"
"  v_mfma_f32_16x16x32_bf16 v[8:11], a[88:91], v[76:79], v[8:11]\n"
"  v_mfma_f32_16x16x32_bf16 v[12:15], a[92:95], v[76:79], v[12:15]\n"
"  s_waitcnt vmcnt(1)\n"
"  v_mfma_f32_16x16x32_bf16 v[8:11], a[96:99], v[80:83], v[8:11]\n"
"  v_mfma_f32_16x16x32_bf16 v[12:15], a[100:103], v[80:83], v[12:15]\n"
"  s_waitcnt vmcnt(0)\n"
"  v_mfma_f32_16x16x32_bf16 v[8:11], a[104:107], v[84:87], v[8:11]\n"
"  v_mfma_f32_16x16x32_bf16 v[12:15], a[108:111], v[84:87], v[12:15]\n"
"  s_lshr_b32 s61, s53, 1\n"
"  s_lshl_b32 s61, s61, 7\n"
"  s_add_u32 s61, s61, 2146304\n"
"  s_cmp_eq_u32 s53, 127\n"
"  s_cselect_b32 s61, 2156544, s61\n"
"  s_cmp_lg_u32 s50, 0\n"
"  s_cbranch_scc1 L_wo%=d\n"
"  s_mov_b32 s59, 0\n"
"L_wo%=:\n"
"  buffer_load_dword v6, v2, s[40:43], s61 offen sc1\n"
"  s_waitcnt vmcnt(0)\n"
"  v_readfirstlane_b32 s48, v6\n"
"  s_nop 3\n"
"  s_cmp_ge_u32 s48, 32\n"
"  s_cbranch_scc1 L_wo%=d\n"
"  s_cmp_lg_u32 s60, 0\n"
"  s_cbranch_scc1 L_wo%=d\n"
"  s_sleep 1\n"
"  s_add_u32 s59, s59, 1\n"
"  s_cmp_gt_u32 s59, 200000\n"
"  s_cselect_b32 s60, 1, 0\n"
"  s_branch L_wo%=\n"
"L_wo%=d:\n"
"  s_barrier\n"
"  s_add_u32 s49, s56, 0\n"
"  buffer_load_dwordx4 v[32:35], v0, s[40:43], s49 offen offset:0 sc1\n"
"  buffer_load_dwordx4 v[36:39], v0, s[40:43], s49 offen offset:1024 sc1\n"
"  buffer_load_dwordx4 v[40:43], v0, s[40:43], s49 offen offset:2048 sc1\n"
"  buffer_load_dwordx4 v[44:47], v0, s[40:43], s49 offen offset:3072 sc1\n"
"  s_add_u32 s49, s56, 4096\n"
"  buffer_load_dwordx4 v[48:51], v0, s[40:43], s49 offen offset:0 sc1\n"
"  buffer_load_dwordx4 v[52:55], v0, s[40:43], s49 offen offset:1024 sc1\n"
"  buffer_load_dwordx4 v[56:59], v0, s[40:43], s49 offen offset:2048 sc1\n"
"  buffer_load_dwordx4 v[60:63], v0, s[40:43], s49 offen offset:3072 sc1\n"
"  s_add_u32 s49, s56, 8192\n"
"  buffer_load_dwordx4 v[64:67], v0, s[40:43], s49 offen offset:0 sc1\n"
"  buffer_load_dwordx4 v[68:71], v0, s[40:43], s49 offen offset:1024 sc1\n"
"  buffer_load_dwordx4 v[72:75], v0, s[40:43], s49 offen offset:2048 sc1\n"
"  buffer_load_dwordx4 v[76:79], v0, s[40:43], s49 offen offset:3072 sc1\n"
"  s_add_u32 s49, s56, 12288\n"
"  buffer_load_dwordx4 v[80:83], v0, s[40:43], s49 offen offset:0 sc1\n"
"  buffer_load_dwordx4 v[84:87], v0, s[40:43], s49 offen offset:1024 sc1\n"
"  buffer_load_dwordx4 v[88:91], v0, s[40:43], s49 offen offset:2048 sc1\n"
"  buffer_load_dwordx4 v[92:95], v0, s[40:43], s49 offen offset:3072 sc1\n"
"  s_add_u32 s49, s56, 16384\n"
"  buffer_load_dwordx4 v[96:99], v0, s[40:43], s49 offen offset:0 sc1\n"
"  buffer_load_dwordx4 v[100:103], v0, s[40:43], s49 offen offset:1024 sc1\n"
"  buffer_load_dwordx4 v[104:107], v0, s[40:43], s49 offen offset:2048 sc1\n"
"  buffer_load_dwordx4 v[108:111], v0, s[40:43], s49 offen offset:3072 sc1\n"
"  s_add_u32 s49, s56, 20480\n"
"  buffer_load_dwordx4 v[112:115], v0, s[40:43], s49 offen offset:0 sc1\n"
"  buffer_load_dwordx4 v[116:119], v0, s[40:43], s49 offen offset:1024 sc1\n"
"  buffer_load_dwordx4 v[120:123], v0, s[40:43], s49 offen offset:2048 sc1\n"
"  buffer_load_dwordx4 v[124:127], v0, s[40:43], s49 offen offset:3072 sc1\n"
"  s_add_u32 s49, s56, 24576\n"
"  buffer_load_dwordx4 v[128:131], v0, s[40:43], s49 offen offset:0 sc1\n"
"  buffer_load_dwordx4 v[132:135], v0, s[40:43], s49 offen offset:1024 sc1\n"
"  buffer_load_dwordx4 v[136:139], v0, s[40:43], s49 offen offset:2048 sc1\n"
"  buffer_load_dwordx4 v[140:143], v0, s[40:43], s49 offen offset:3072 sc1\n"
"  s_waitcnt vmcnt(27)\n"
"  v_mfma_f32_16x16x32_bf16 v[8:11], a[112:115], v[32:35], v[8:11]\n"
"  v_mfma_f32_16x16x32_bf16 v[12:15], a[116:119], v[32:35], v[12:15]\n"
"  s_add_u32 s49, s56, 28672\n"
"  buffer_load_dwordx4 v[32:35], v0, s[40:43], s49 offen offset:0 sc1\n"
"  s_waitcnt vmcnt(27)\n"
"  v_mfma_f32_16x16x32_bf16 v[8:11], a[120:123], v[36:39], v[8:11]\n"
"  v_mfma_f32_16x16x32_bf16 v[12:15], a[124:127], v[36:39], v[12:15]\n"
"  buffer_load_dwordx4 v[36:39], v0, s[40:43], s49 offen offset:1024 sc1\n"
"  s_waitcnt vmcnt(27)\n"
"  v_mfma_f32_16x16x32_bf16 v[8:11], a[128:131], v[40:43], v[8:11]\n"
"  v_mfma_f32_16x16x32_bf16 v[12:15], a[132:135], v[40:43], v[12:15]\n"
"  buffer_load_dwordx4 v[40:43], v0, s[40:43], s49 offen offset:2048 sc1\n"
"  s_waitcnt vmcnt(27)\n"
"  v_mfma_f32_16x16x32_bf16 v[8:11], a[136:139], v[44:47], v[8:11]\n"
"  v_mfma_f32_16x16x32_bf16 v[12:15], a[140:143], v[44:47], v[12:15]\n"
"  buffer_load_dwordx4 v[44:47], v0, s[40:43], s49 offen offset:3072 sc1\n"
"  s_waitcnt vmcnt(27)\n"
"  v_mfma_f32_16x16x32_bf16 v[8:11], a[144:147], v[48:51], v[8:11]\n"
"  v_mfma_f32_16x16x32_bf16 v[12:15], a[148:151], v[48:51], v[12:15]\n"
"  s_waitcnt vmcnt(26)\n"
"  v_mfma_f32_16x16x32_bf16 v[8:11], a[152:155], v[52:55], v[8:11]\n"
"  v_mfma_f32_16x16x32_bf16 v[12:15], a[156:159], v[52:55], v[12:15]\n"
"  s_waitcnt vmcnt(25)\n"
"  v_mfma_f32_16x16x32_bf16 v[8:11], a[160:163], v[56:59], v[8:11]\n"
"  v_mfma_f32_16x16x32_bf16 v[12:15], a[164:167], v[56:59], v[12:15]\n"
"  s_waitcnt vmcnt(24)\n"
"  v_mfma_f32_16x16x32_bf16 v[8:11], a[168:171], v[60:63], v[8:11]\n"
"  v_mfma_f32_16x16x32_bf16 v[12:15], a[172:175], v[60:63], v[12:15]\n"
"  s_waitcnt vmcnt(23)\n"
"  v_mfma_f32_16x16x32_bf16 v[8:11], a[176:179], v[64:67], v[8:11]\n"
"  v_mfma_f32_16x16x32_bf16 v[12:15], a[180:183], v[64:67], v[12:15]\n"
"  s_waitcnt vmcnt(22)\n"
"  v_mfma_f32_16x16x32_bf16 v[8:11], a[184:187], v[68:71], v[8:11]\n"
"  v_mfma_f32_16x16x32_bf16 v[12:15], a[188:191], v[68:71], v[12:15]\n"
"  s_waitcnt vmcnt(21)\n"
"  v_mfma_f32_16x16x32_bf16 v[8:11], a[192:195], v[72:75], v[8:11]\n"
"  v_mfma_f32_16x16x32_bf16 v[12:15], a[196:199], v[72:75], v[12:15]\n"
"  s_waitcnt vmcnt(20)\n"
"  v_mfma_f32_16x16x32_bf16 v[8:11], a[200:203], v[76:79], v[8:11]\n"
"  v_mfma_f32_16x16x32_bf16 v[12:15], a[204:207], v[76:79], v[12:15]\n"
"  s_waitcnt vmcnt(19)\n"
"  v_mfma_f32_16x16x32_bf16 v[8:11], a[208:211], v[80:83], v[8:11]\n"
"  v_mfma_f32_16x16x32_bf16 v[12:15], a[212:215], v[80:83], v[12:15]\n"
"  s_waitcnt vmcnt(18)\n"
"  v_mfma_f32_16x16x32_bf16 v[8:11], a[216:219], v[84:87], v[8:11]\n"
"  v_mfma_f32_16x16x32_bf16 v[12:15], a[220:223], v[84:87], v[12:15]\n"
"  s_waitcnt vmcnt(17)\n"
"  v_mfma_f32_16x16x32_bf16 v[8:11], a[224:227], v[88:91], v[8:11]\n"
"  v_mfma_f32_16x16x32_bf16 v[12:15], a[228:231], v[88:91], v[12:15]\n"
"  s_waitcnt vmcnt(16)\n"
"  v_mfma_f32_16x16x32_bf16 v[8:11], a[232:235], v[92:95], v[8:11]\n"
"  v_mfma_f32_16x16x32_bf16 v[12:15], a[236:239], v[92:95], v[12:15]\n"
"  s_waitcnt vmcnt(15)\n"
"  v_mfma_f32_16x16x32_bf16 v[8:11], a[240:243], v[96:99], v[8:11]\n"
"  v_mfma_f32_16x16x32_bf16 v[12:15], a[244:247], v[96:99], v[12:15]\n"
"  s_waitcnt vmcnt(14)\n"
"  v_mfma_f32_16x16x32_bf16 v[8:11], a[248:251], v[100:103], v[8:11]\n"
"  v_mfma_f32_16x16x32_bf16 v[12:15], a[252:255], v[100:103], v[12:15]\n"
"  s_waitcnt vmcnt(13)\n"
"  v_mfma_f32_16x16x32_bf16 v[8:11], v[144:147], v[104:107], v[8:11]\n"
"  v_mfma_f32_16x16x32_bf16 v[12:15], v[148:151], v[104:107], v[12:15]\n"
"  s_waitcnt vmcnt(12)\n"
"  v_mfma_f32_16x16x32_bf16 v[8:11], v[152:155], v[108:111], v[8:11]\n"
"  v_mfma_f32_16x16x32_bf16 v[12:15], v[156:159], v[108:111], v[12:15]\n"
"  s_waitcnt vmcnt(11)\n"
"  v_mfma_f32_16x16x32_bf16 v[8:11], v[160:163], v[112:115], v[8:11]\n"
"  v_mfma_f32_16x16x32_bf16 v[12:15], v[164:167], v[112:115], v[12:15]\n"
"  s_waitcnt vmcnt(10)\n"
"  v_mfma_f32_16x16x32_bf16 v[8:11], v[168:171], v[116:119], v[8:11]\n"
"  v_mfma_f32_16x16x32_bf16 v[12:15], v[172:175], v[116:119], v[12:15]\n"
"  s_waitcnt vmcnt(9)\n"
"  v_mfma_f32_16x16x32_bf16 v[8:11], v[176:179], v[120:123], v[8:11]\n"
"  v_mfma_f32_16x16x32_bf16 v[12:15], v[180:183], v[120:123], v[12:15]\n"
"  s_waitcnt vmcnt(8)\n"
"  v_mfma_f32_16x16x32_bf16 v[8:11], v[184:187], v[124:127], v[8:11]\n"
"  v_mfma_f32_16x16x32_bf16 v[12:15], v[188:191], v[124:127], v[12:15]\n"
"  s_waitcnt vmcnt(7)\n"
"  v_mfma_f32_16x16x32_bf16 v[8:11], v[192:195], v[128:131], v[8:11]\n"
"  v_mfma_f32_16x16x32_bf16 v[12:15], v[196:199], v[128:131], v[12:15]\n"
"  s_waitcnt vmcnt(6)\n"
"  v_mfma_f32_16x16x32_bf16 v[8:11], v[200:203], v[132:135], v[8:11]\n"
"  v_mfma_f32_16x16x32_bf16 v[12:15], v[204:207], v[132:135], v[12:15]\n"
"  s_waitcnt vmcnt(5)\n"
"  v_mfma_f32_16x16x32_bf16 v[8:11], v[208:211], v[136:139], v[8:11]\n"
"  v_mfma_f32_16x16x32_bf16 v[12:15], v[212:215], v[136:139], v[12:15]\n"
"  s_waitcnt vmcnt(4)\n"
"  v_mfma_f32_16x16x32_bf16 v[8:11], v[216:219], v[140:143], v[8:11]\n"
"  v_mfma_f32_16x16x32_bf16 v[12:15], v[220:223], v[140:143], v[12:15]\n"
"  s_waitcnt vmcnt(3)\n"
"  v_mfma_f32_16x16x32_bf16 v[8:11], v[224:227], v[32:35], v[8:11]\n"
"  v_mfma_f32_16x16x32_bf16 v[12:15], v[228:231], v[32:35], v[12:15]\n"
"  s_waitcnt vmcnt(2)\n"
"  v_mfma_f32_16x16x32_bf16 v[8:11], v[232:235], v[36:39], v[8:11]\n"
"  v_mfma_f32_16x16x32_bf16 v[12:15], v[236:239], v[36:39], v[12:15]\n"
"  s_waitcnt vmcnt(1)\n"
"  v_mfma_f32_16x16x32_bf16 v[8:11], v[240:243], v[40:43], v[8:11]\n"
"  v_mfma_f32_16x16x32_bf16 v[12:15], v[244:247], v[40:43], v[12:15]\n"
"  s_waitcnt vmcnt(0)\n"
"  v_mfma_f32_16x16x32_bf16 v[8:11], v[248:251], v[44:47], v[8:11]\n"
"  v_mfma_f32_16x16x32_bf16 v[12:15], v[252:255], v[44:47], v[12:15]\n"
"  s_nop 6\n"
"  ds_write_b128 v4, v[8:11]\n"
"  ds_write_b128 v4, v[12:15] offset:1024\n"
"  v_lshlrev_b32 v20, 16, v16\n"
"  v_and_b32 v21, 0xffff0000, v16\n"
"  v_lshlrev_b32 v22, 16, v17\n"
"  v_and_b32 v23, 0xffff0000, v17\n"
"  s_waitcnt lgkmcnt(0)\n"
"  s_barrier\n"
"  ds_read_b128 v[32:35], v5 offset:0\n"
"  ds_read_b128 v[36:39], v5 offset:2048\n"
"  ds_read_b128 v[40:43], v5 offset:4096\n"
"  ds_read_b128 v[44:47], v5 offset:6144\n"
"  s_waitcnt lgkmcnt(3)\n"
"  v_add_f32 v20, v20, v32\n"
"  v_add_f32 v21, v21, v33\n"
"  v_add_f32 v22, v22, v34\n"
"  v_add_f32 v23, v23, v35\n"
"  s_waitcnt lgkmcnt(2)\n"
"  v_add_f32 v20, v20, v36\n"
"  v_add_f32 v21, v21, v37\n"
"  v_add_f32 v22, v22, v38\n"
"  v_add_f32 v23, v23, v39\n"
"  s_waitcnt lgkmcnt(1)\n"
"  v_add_f32 v20, v20, v40\n"
"  v_add_f32 v21, v21, v41\n"
"  v_add_f32 v22, v22, v42\n"
"  v_add_f32 v23, v23, v43\n"
"  s_waitcnt lgkmcnt(0)\n"
"  v_add_f32 v20, v20, v44\n"
"  v_add_f32 v21, v21, v45\n"
"  v_add_f32 v22, v22, v46\n"
"  v_add_f32 v23, v23, v47\n"
"  v_cvt_pk_bf16_f32 v24, v20, v21\n"
"  v_cvt_pk_bf16_f32 v25, v22, v23\n"
"  buffer_store_dwordx2 v[24:25], v3, s[68:71], s58 offen\n"
"  s_add_u32 s53, s53, 8\n"
"  s_cmp_lt_u32 s53, 128\n"
"  s_cbranch_scc1 L_p4%=\n"
"  s_waitcnt vmcnt(0)\n"
            :: "s"(ap->ws), "s"(ap->Wo), "s"(ap->Wd), "s"(ap->x), "s"(ap->out), "s"(i & 15), "s"((x - 4) * 2 + (i >> 4)), "s"(w)
            : BULK_CLOB);
    } else {
        ARGS();
        const int lane = __builtin_amdgcn_mbcnt_hi(-1, __builtin_amdgcn_mbcnt_lo(-1, 0));
        char* ws = ap->ws;
        const unsigned tag = __builtin_amdgcn_readfirstlane(*(const unsigned*)(ws + OFF_CTL + C_EP)) + 1u;
        setup_share(ap->x, ap->mem0, ws, rank * 4 + w, tag);
        ARGS(); TS(0);
        {
            u32x4 acc = {0u, 0u, 0u, 0u};
            const __amdgpu_buffer_rsrc_t r = __builtin_amdgcn_make_buffer_rsrc((void*)ap->Wq, 0, 512 * 4096 * 2, 0x00020000);
            const __amdgpu_buffer_rsrc_t r2 = __builtin_amdgcn_make_buffer_rsrc((void*)ap->Wo, 0, 512 * 4096 * 2, 0x00020000);
            unsigned va = ((slot - 2) * 256 + (h & 7) * 32 + w * 8) * 8192 + x * 2048 + lane * 16;
            unsigned vb = h * 131072 + (slot - 2) * 65536 + w * 16384 + lane * 16;
#pragma unroll
            for (int k = 0; k < 16; ++k) { acc ^= __builtin_amdgcn_raw_buffer_load_b128(r, va, 0, 0); va += (k & 1) ? 7168 : 1024; asm("" : "+v"(va)); }
#pragma unroll
            for (int k = 0; k < 16; ++k) { acc ^= __builtin_amdgcn_raw_buffer_load_b128(r2, vb, 0, 0); vb += 1024; asm("" : "+v"(vb)); }
            asm volatile("" :: "v"(acc));
        }
        TS(1);
        const __amdgpu_buffer_rsrc_t rs = __builtin_amdgcn_make_buffer_rsrc(ws, 0, 0x0f000000, 0x00020000);
        __builtin_amdgcn_s_barrier();
        unsigned long long vote = 0;
        if (w == 3) {
            unsigned best0 = ~0u, best1 = ~0u;
            for (int r = 0; r < 4; ++r) {
                unsigned* p = (unsigned*)(ws + OFF_XCH + (118 + slot + (i & 1) * 2) * 16384 + (r & 1) * 8192 + (h >> 1) * 128);
                unsigned long long t0, t1;
                asm volatile("s_memtime %0\n\ts_waitcnt lgkmcnt(0)\n\tglobal_store_dword %2, %3, off sc1\n\ts_waitcnt vmcnt(0)\n\ts_memtime %1\n\ts_waitcnt lgkmcnt(0)"
                             : "=&s"(t0), "=&s"(t1) : "v"(p), "v"(SENT) : "memory");
                const unsigned d = (unsigned)(t1 - t0);
                if (r & 1) best1 = min(best1, d); else best0 = min(best0, d);
            }
            vote = ((unsigned long long)min(min(best0, best1), 1023u) << (((x ^ slot) & 1) * 16)) | ((unsigned long long)(256u + (best1 < best0 ? 1u : 0u)) << 32);
        }
        if (w == 3) for (unsigned sp = 0; sp < 100000u; ++sp) {
            const u32x4 dn = __builtin_amdgcn_raw_buffer_load_b128(rs, lane * 16, OFF_CTL + C_DONE, SC1);
            if (__all(dn[0] == tag && dn[1] == tag && dn[2] == tag && dn[3] == tag)) break;
            __builtin_amdgcn_s_sleep(16); asm volatile("" ::: "memory");
        }
        if (w == 3 && lane == 0) {
            __hip_atomic_fetch_add((unsigned long long*)(ws + OFF_CTL + C_VOTE), vote, __ATOMIC_RELAXED, __HIP_MEMORY_SCOPE_AGENT);
        }
        __builtin_amdgcn_s_barrier();
        asm volatile("" ::: "memory");
        TS(2);
        attn_role(ws, ((slot - 2) * 4 + w) * 32 + h);
        __builtin_amdgcn_raw_buffer_store_b32(tag, rs, (rank | w | lane) == 0 ? 0u : 0x7f000000u, OFF_CTL + C_EP, SC1);
        __builtin_amdgcn_raw_buffer_store_b32(tag - 1u, rs, lane == 0 ? (rank * 4 + w) * 4u : 0x7f000000u, OFF_CTL + C_DONE, SC1);
    }
    ARGS(); TS(3);
    return;
    }
    {
        const __attribute__((address_space(4))) Args* cp = (const __attribute__((address_space(4))) Args*)__builtin_amdgcn_kernarg_segment_ptr();
        asm volatile("" : "+s"(cp), "+s"(b));
        const int h = (b & 3) * 8 + ((b >> 3) & 7), slot = b >> 6;
        asm volatile(
"  v_mbcnt_lo_u32_b32 v60, -1, 0\n"
"  v_mbcnt_hi_u32_b32 v60, -1, v60\n"
"  s_getpc_b64 s[72:73]\n"
"L_cs%=:\n"
"  s_mov_b32 s74, L_ce%=-L_cs%=\n"
"  s_mov_b32 s75, 0x00020000\n"
"  v_lshlrev_b32 v122, 7, v60\n"
"  buffer_load_dword v123, v122, s[72:75], 0 offen\n"
"  v_add_u32 v122, 0x2000, v122\n"
"  buffer_load_dword v123, v122, s[72:75], 0 offen\n"
"  v_add_u32 v122, 0x2000, v122\n"
"  buffer_load_dword v123, v122, s[72:75], 0 offen\n"
"  s_load_dwordx8 s[24:31], %0, 8\n"
"  s_load_dwordx2 s[40:41], %0, 72\n"
"  s_load_dwordx4 s[32:35], %0, 80\n"
"  s_mov_b32 s42, 0x0f000000\n"
"  s_mov_b32 s43, 0x00020000\n"
"  s_mov_b32 s64, %1\n"
"  s_mov_b32 s63, %2\n"
"  s_mov_b32 s51, %3\n"
"  s_lshl_b32 s53, s51, 5\n"
"  s_add_u32 s53, s53, s64\n"
"  s_waitcnt lgkmcnt(0)\n"
"  s_mov_b32 s61, s32\n"
"  s_mov_b32 s62, s33\n"
"  s_mov_b32 s101, s35\n"
"  s_mov_b32 s36, 0x10000\n"
"  s_mov_b32 s37, 0x10000\n"
"  s_mov_b32 s38, 2000\n"
"  s_mov_b32 s34, 0\n"
"  s_mov_b32 s35, 0\n"
"  s_waitcnt lgkmcnt(0)\n"
"  s_memtime s[32:33]\n"
"  s_memrealtime s[72:73]\n"
"  s_nop 2\n"
"  s_memrealtime s[74:75]\n"
"  s_nop 2\n"
"  s_memrealtime s[76:77]\n"
"  s_nop 2\n"
"  s_memrealtime s[78:79]\n"
"  s_nop 2\n"
"  s_memrealtime s[80:81]\n"
"  s_nop 2\n"
"  s_memrealtime s[82:83]\n"
"  s_nop 2\n"
"  s_memrealtime s[46:47]\n"
"  s_nop 2\n"
"  s_memrealtime s[88:89]\n"
"  s_waitcnt lgkmcnt(0)\n"
"  s_mov_b32 s68, 0\n"
"  s_cmp_lg_u32 s88, s72\n"
"  s_cselect_b32 s69, s88, s69\n"
"  s_cselect_b32 s68, 112, s68\n"
"  s_cmp_lg_u32 s46, s72\n"
"  s_cselect_b32 s69, s46, s69\n"
"  s_cselect_b32 s68, 96, s68\n"
"  s_cmp_lg_u32 s82, s72\n"
"  s_cselect_b32 s69, s82, s69\n"
"  s_cselect_b32 s68, 80, s68\n"
"  s_cmp_lg_u32 s80, s72\n"
"  s_cselect_b32 s69, s80, s69\n"
"  s_cselect_b32 s68, 64, s68\n"
"  s_cmp_lg_u32 s78, s72\n"
"  s_cselect_b32 s69, s78, s69\n"
"  s_cselect_b32 s68, 48, s68\n"
"  s_cmp_lg_u32 s76, s72\n"
"  s_cselect_b32 s69, s76, s69\n"
"  s_cselect_b32 s68, 32, s68\n"
"  s_cmp_lg_u32 s74, s72\n"
"  s_cselect_b32 s69, s74, s69\n"
"  s_cselect_b32 s68, 16, s68\n"
"  s_cmp_eq_u32 s68, 0\n"
"  s_cbranch_scc1 L_an0%=\n"
"  s_add_u32 s68, s68, s32\n"
"  s_mul_i32 s69, s69, 24\n"
"  s_mov_b32 s35, s68\n"
"  s_mov_b32 s34, s69\n"
"L_an0%=:\n"
"  s_mov_b32 s47, 0x00020000\n"
"  s_mov_b32 s60, 0\n"
"  s_mov_b32 s65, 0xff80ffff\n"
"  s_mov_b32 s66, 0xff81ff81\n"
"  s_mov_b32 s67, 0x3e0293ee\n"
"  v_mbcnt_lo_u32_b32 v60, -1, 0\n"
"  v_mbcnt_hi_u32_b32 v60, -1, v60\n"
"  v_and_b32 v61, 15, v60\n"
"  v_lshrrev_b32 v62, 4, v60\n"
"  v_lshlrev_b32 v0, 4, v60\n"
"  s_add_u32 s88, s40, 2162688\n"
"  s_addc_u32 s89, s41, 0\n"
"  s_load_dword s90, s[88:89], 0\n"
"  v_lshrrev_b32 v63, 2, v60\n"
"  v_and_b32 v64, 3, v60\n"
"  v_lshlrev_b32 v64, 4, v64\n"
"  v_lshl_add_u32 v65, v63, 13, v64\n"
"  v_lshl_add_u32 v66, v63, 10, v64\n"
"  s_lshl_b32 s68, s63, 15\n"
"  s_add_u32 s91, s68, 32768\n"
"  v_add_u32 v16, s91, v0\n"
"  v_bfe_u32 v69, v60, 2, 1\n"
"  v_bfe_u32 v70, v60, 3, 1\n"
"  v_bfe_u32 v71, v60, 4, 1\n"
"  v_bfe_u32 v72, v60, 5, 1\n"
"  v_and_b32 v63, 3, v60\n"
"  v_lshlrev_b32 v63, 3, v63\n"
"  v_add_u32 v63, s91, v63\n"
"  v_lshl_add_u32 v64, v69, 10, v63\n"
"  v_lshl_add_u32 v64, v72, 8, v64\n"
"  v_lshl_add_u32 v64, v70, 7, v64\n"
"  v_lshl_add_u32 v67, v71, 6, v64\n"
"  v_lshl_add_u32 v64, v71, 10, v63\n"
"  v_lshl_add_u32 v64, v72, 9, v64\n"
"  v_lshl_add_u32 v64, v70, 8, v64\n"
"  v_lshl_add_u32 v68, v69, 6, v64\n"
"  s_lshl_b32 s92, s64, 8\n"
"  s_lshl_b32 s68, s63, 6\n"
"  s_add_u32 s92, s92, s68\n"
"  s_lshl_b32 s93, s64, 17\n"
"  s_lshl_b32 s68, s63, 7\n"
"  s_add_u32 s93, s93, s68\n"
"  s_lshl_b32 s68, s51, 9\n"
"  s_add_u32 s93, s93, s68\n"
"  s_mov_b32 s46, 4194304\n"
"  s_lshl_b32 s68, s63, 20\n"
"  s_add_u32 s92, s92, s68\n"
"  v_add_u32 v33, 130048, v65\n"
"  v_add_u32 v56, 15360, v66\n"
"  v_add_u32 v34, 260096, v65\n"
"  v_add_u32 v57, 30720, v66\n"
"  v_add_u32 v35, 390144, v65\n"
"  v_add_u32 v58, 46080, v66\n"
"  s_lshl_b32 s69, s51, 21\n"
"  s_lshl_b32 s68, s63, 16\n"
"  s_add_u32 s69, s69, s68\n"
"  s_and_b32 s68, s64, 7\n"
"  s_lshl_b32 s68, s68, 18\n"
"  s_add_u32 s69, s69, s68\n"
"  s_lshr_b32 s68, s64, 3\n"
"  s_lshl_b32 s68, s68, 11\n"
"  s_add_u32 s69, s69, s68\n"
"  s_mov_b64 s[44:45], s[26:27]\n"
"  s_add_u32 s68, s69, 0\n"
"  buffer_load_dwordx4 v[124:127], v0, s[44:47], s68 offen\n"
"  buffer_load_dwordx4 v[124:127], v0, s[44:47], s68 offen offset:1024\n"
"  s_add_u32 s68, s69, 8192\n"
"  buffer_load_dwordx4 v[124:127], v0, s[44:47], s68 offen\n"
"  buffer_load_dwordx4 v[124:127], v0, s[44:47], s68 offen offset:1024\n"
"  s_add_u32 s68, s69, 16384\n"
"  buffer_load_dwordx4 v[124:127], v0, s[44:47], s68 offen\n"
"  buffer_load_dwordx4 v[124:127], v0, s[44:47], s68 offen offset:1024\n"
"  s_add_u32 s68, s69, 24576\n"
"  buffer_load_dwordx4 v[124:127], v0, s[44:47], s68 offen\n"
"  buffer_load_dwordx4 v[124:127], v0, s[44:47], s68 offen offset:1024\n"
"  s_add_u32 s68, s69, 32768\n"
"  buffer_load_dwordx4 v[124:127], v0, s[44:47], s68 offen\n"
"  buffer_load_dwordx4 v[124:127], v0, s[44:47], s68 offen offset:1024\n"
"  s_add_u32 s68, s69, 40960\n"
"  buffer_load_dwordx4 v[124:127], v0, s[44:47], s68 offen\n"
"  buffer_load_dwordx4 v[124:127], v0, s[44:47], s68 offen offset:1024\n"
"  s_add_u32 s68, s69, 49152\n"
"  buffer_load_dwordx4 v[124:127], v0, s[44:47], s68 offen\n"
"  buffer_load_dwordx4 v[124:127], v0, s[44:47], s68 offen offset:1024\n"
"  s_add_u32 s68, s69, 57344\n"
"  buffer_load_dwordx4 v[124:127], v0, s[44:47], s68 offen\n"
"  buffer_load_dwordx4 v[124:127], v0, s[44:47], s68 offen offset:1024\n"
"  s_mov_b64 s[44:45], s[28:29]\n"
"  s_add_u32 s68, s69, 0\n"
"  buffer_load_dwordx4 v[124:127], v0, s[44:47], s68 offen\n"
"  buffer_load_dwordx4 v[124:127], v0, s[44:47], s68 offen offset:1024\n"
"  s_add_u32 s68, s69, 8192\n"
"  buffer_load_dwordx4 v[124:127], v0, s[44:47], s68 offen\n"
"  buffer_load_dwordx4 v[124:127], v0, s[44:47], s68 offen offset:1024\n"
"  s_add_u32 s68, s69, 16384\n"
"  buffer_load_dwordx4 v[124:127], v0, s[44:47], s68 offen\n"
"  buffer_load_dwordx4 v[124:127], v0, s[44:47], s68 offen offset:1024\n"
"  s_add_u32 s68, s69, 24576\n"
"  buffer_load_dwordx4 v[124:127], v0, s[44:47], s68 offen\n"
"  buffer_load_dwordx4 v[124:127], v0, s[44:47], s68 offen offset:1024\n"
"  s_add_u32 s68, s69, 32768\n"
"  buffer_load_dwordx4 v[124:127], v0, s[44:47], s68 offen\n"
"  buffer_load_dwordx4 v[124:127], v0, s[44:47], s68 offen offset:1024\n"
"  s_add_u32 s68, s69, 40960\n"
"  buffer_load_dwordx4 v[124:127], v0, s[44:47], s68 offen\n"
"  buffer_load_dwordx4 v[124:127], v0, s[44:47], s68 offen offset:1024\n"
"  s_add_u32 s68, s69, 49152\n"
"  buffer_load_dwordx4 v[124:127], v0, s[44:47], s68 offen\n"
"  buffer_load_dwordx4 v[124:127], v0, s[44:47], s68 offen offset:1024\n"
"  s_add_u32 s68, s69, 57344\n"
"  buffer_load_dwordx4 v[124:127], v0, s[44:47], s68 offen\n"
"  buffer_load_dwordx4 v[124:127], v0, s[44:47], s68 offen offset:1024\n"
"  s_waitcnt vmcnt(4)\n"
"  s_waitcnt lgkmcnt(0)\n"
"  s_add_u32 s90, s90, 1\n"
"  s_mov_b64 s[44:45], s[26:27]\n"
"  s_add_u32 m0, s91, 0\n"
"  s_xor_b32 s68, s92, 2097152\n"
"  buffer_load_dwordx4 v65, s[44:47], s68 offen lds\n"
"  buffer_load_dwordx4 v33, s[44:47], s68 offen offset:1024 lds\n"
"  buffer_load_dwordx4 v34, s[44:47], s68 offen offset:2048 lds\n"
"  buffer_load_dwordx4 v35, s[44:47], s68 offen offset:3072 lds\n"
"  s_add_u32 m0, s91, 4096\n"
"  s_xor_b32 s68, s92, 3145728\n"
"  buffer_load_dwordx4 v65, s[44:47], s68 offen lds\n"
"  buffer_load_dwordx4 v33, s[44:47], s68 offen offset:1024 lds\n"
"  buffer_load_dwordx4 v34, s[44:47], s68 offen offset:2048 lds\n"
"  buffer_load_dwordx4 v35, s[44:47], s68 offen offset:3072 lds\n"
"  s_add_u32 m0, s91, 8192\n"
"  s_xor_b32 s68, s92, 2621440\n"
"  buffer_load_dwordx4 v65, s[44:47], s68 offen lds\n"
"  buffer_load_dwordx4 v33, s[44:47], s68 offen offset:1024 lds\n"
"  buffer_load_dwordx4 v34, s[44:47], s68 offen offset:2048 lds\n"
"  buffer_load_dwordx4 v35, s[44:47], s68 offen offset:3072 lds\n"
"  s_add_u32 m0, s91, 12288\n"
"  s_xor_b32 s68, s92, 3670016\n"
"  buffer_load_dwordx4 v65, s[44:47], s68 offen lds\n"
"  buffer_load_dwordx4 v33, s[44:47], s68 offen offset:1024 lds\n"
"  buffer_load_dwordx4 v34, s[44:47], s68 offen offset:2048 lds\n"
"  buffer_load_dwordx4 v35, s[44:47], s68 offen offset:3072 lds\n"
"  s_mov_b64 s[44:45], s[28:29]\n"
"  s_add_u32 m0, s91, 16384\n"
"  s_xor_b32 s68, s92, 0\n"
"  buffer_load_dwordx4 v65, s[44:47], s68 offen lds\n"
"  buffer_load_dwordx4 v33, s[44:47], s68 offen offset:1024 lds\n"
"  buffer_load_dwordx4 v34, s[44:47], s68 offen offset:2048 lds\n"
"  buffer_load_dwordx4 v35, s[44:47], s68 offen offset:3072 lds\n"
"  s_add_u32 m0, s91, 20480\n"
"  s_xor_b32 s68, s92, 1048576\n"
"  buffer_load_dwordx4 v65, s[44:47], s68 offen lds\n"
"  buffer_load_dwordx4 v33, s[44:47], s68 offen offset:1024 lds\n"
"  buffer_load_dwordx4 v34, s[44:47], s68 offen offset:2048 lds\n"
"  buffer_load_dwordx4 v35, s[44:47], s68 offen offset:3072 lds\n"
"  s_add_u32 m0, s91, 24576\n"
"  s_xor_b32 s68, s92, 524288\n"
"  buffer_load_dwordx4 v65, s[44:47], s68 offen lds\n"
"  buffer_load_dwordx4 v33, s[44:47], s68 offen offset:1024 lds\n"
"  buffer_load_dwordx4 v34, s[44:47], s68 offen offset:2048 lds\n"
"  buffer_load_dwordx4 v35, s[44:47], s68 offen offset:3072 lds\n"
"  s_add_u32 m0, s91, 28672\n"
"  s_xor_b32 s68, s92, 1572864\n"
"  buffer_load_dwordx4 v65, s[44:47], s68 offen lds\n"
"  buffer_load_dwordx4 v33, s[44:47], s68 offen offset:1024 lds\n"
"  buffer_load_dwordx4 v34, s[44:47], s68 offen offset:2048 lds\n"
"  buffer_load_dwordx4 v35, s[44:47], s68 offen offset:3072 lds\n"
"  s_lshl_b32 s68, s63, 11\n"
"  v_add_u32 v1, s68, v0\n"
"  s_xor_b32 s69, s63, 1\n"
"  s_lshl_b32 s69, s69, 11\n"
"  v_add_u32 v2, s69, v0\n"
"  s_xor_b32 s69, s63, 2\n"
"  s_lshl_b32 s69, s69, 11\n"
"  v_add_u32 v3, s69, v0\n"
"  s_xor_b32 s69, s63, 3\n"
"  s_lshl_b32 s69, s69, 11\n"
"  v_add_u32 v4, s69, v0\n"
"  v_and_b32 v64, 1, v61\n"
"  v_lshrrev_b32 v73, 2, v61\n"
"  v_lshl_add_u32 v73, v73, 1, v64\n"
"  v_bfe_u32 v74, v61, 1, 1\n"
"  v_lshrrev_b32 v63, 2, v61\n"
"  v_lshlrev_b32 v63, 8, v63\n"
"  v_lshl_add_u32 v63, v64, 6, v63\n"
"  v_lshl_add_u32 v63, v72, 7, v63\n"
"  v_lshl_add_u32 v63, v71, 5, v63\n"
"  v_lshl_add_u32 v63, v74, 4, v63\n"
"  v_add_u32 v5, s68, v63\n"
"  v_lshlrev_b32 v6, 8, v60\n"
"  v_lshrrev_b32 v63, 5, v60\n"
"  v_lshl_add_u32 v6, v63, 13, v6\n"
"  v_mov_b32 v15, 0x4444\n"
"  v_mov_b32 v63, 0xeeee\n"
"  v_cmp_eq_u32 vcc, 1, v74\n"
"  s_nop 1\n"
"  v_cndmask_b32 v15, v15, v63, vcc\n"
"  v_mul_u32_u24 v63, 288, v73\n"
"  v_lshl_add_u32 v63, v62, 5, v63\n"
"  v_lshl_add_u32 v63, v74, 4, v63\n"
"  v_add_u32 v12, 24576, v63\n"
"  v_lshrrev_b32 v63, 2, v61\n"
"  v_lshl_add_u32 v63, v63, 1, v64\n"
"  v_mul_u32_u24 v63, 288, v63\n"
"  v_lshl_add_u32 v63, v62, 4, v63\n"
"  v_add_u32 v10, 29184, v63\n"
"  v_and_b32 v63, 7, v61\n"
"  v_mul_u32_u24 v63, 288, v63\n"
"  v_lshl_add_u32 v63, v62, 4, v63\n"
"  v_add_u32 v9, 26880, v63\n"
"  v_mul_u32_u24 v63, 576, v62\n"
"  v_lshl_add_u32 v63, v61, 2, v63\n"
"  s_lshl_b32 s69, s63, 6\n"
"  v_add_u32 v63, s69, v63\n"
"  v_add_u32 v7, 26880, v63\n"
"  v_add_u32 v8, 29184, v63\n"
"  v_mul_u32_u24 v63, 288, v61\n"
"  v_lshl_add_u32 v63, v62, 4, v63\n"
"  v_add_u32 v63, s69, v63\n"
"  v_add_u32 v11, 24576, v63\n"
"  v_lshlrev_b32 v63, 15, v62\n"
"  v_lshrrev_b32 v64, 3, v61\n"
"  v_lshl_add_u32 v63, v64, 12, v63\n"
"  v_and_b32 v64, 7, v61\n"
"  v_lshl_add_u32 v63, v64, 4, v63\n"
"  s_lshr_b32 s68, s63, 1\n"
"  s_lshl_b32 s68, s68, 18\n"
"  s_lshl_b32 s69, s64, 7\n"
"  s_add_u32 s68, s68, s69\n"
"  s_lshl_b32 s69, s51, 17\n"
"  s_add_u32 s68, s68, s69\n"
"  v_add_u32 v13, s68, v63\n"
"  v_mov_b32 v64, 0x7f000000\n"
"  v_cmp_eq_u32 vcc, 0, v60\n"
"  s_nop 1\n"
"  v_cndmask_b32 v14, v64, v60, vcc\n"
"  v_cmp_gt_u32 vcc, 32, v60\n"
"  v_lshlrev_b32 v63, 7, v60\n"
"  s_and_b32 s68, s63, 1\n"
"  s_lshl_b32 s68, s68, 13\n"
"  s_cmp_ge_u32 s63, 2\n"
"  s_cselect_b32 s68, s68, 0x7f000000\n"
"  v_add_u32 v17, s68, v63\n"
"  v_mov_b32 v20, 0x3f803f80\n"
"  v_mov_b32 v21, 0x3f803f80\n"
"  v_mov_b32 v22, 0x3f803f80\n"
"  v_mov_b32 v23, 0x3f803f80\n"
"  v_mov_b32 v25, 0\n"
"  v_mov_b32 v27, 0\n"
"  v_mov_b32 v29, 0\n"
"  v_and_b32 v61, 7, v61\n"
"  v_mov_b32 v36, 0\n"
"  v_mov_b32 v63, 0x3f80\n"
"  v_cmp_eq_u32 vcc, 0, v61\n"
"  s_nop 1\n"
"  v_cndmask_b32 v36, v36, v63, vcc\n"
"  v_mov_b32 v63, 0x3f800000\n"
"  v_cmp_eq_u32 vcc, 1, v61\n"
"  s_nop 1\n"
"  v_cndmask_b32 v36, v36, v63, vcc\n"
"  v_mov_b32 v37, 0\n"
"  v_mov_b32 v63, 0x3f80\n"
"  v_cmp_eq_u32 vcc, 2, v61\n"
"  s_nop 1\n"
"  v_cndmask_b32 v37, v37, v63, vcc\n"
"  v_mov_b32 v63, 0x3f800000\n"
"  v_cmp_eq_u32 vcc, 3, v61\n"
"  s_nop 1\n"
"  v_cndmask_b32 v37, v37, v63, vcc\n"
"  v_mov_b32 v38, 0\n"
"  v_mov_b32 v63, 0x3f80\n"
"  v_cmp_eq_u32 vcc, 4, v61\n"
"  s_nop 1\n"
"  v_cndmask_b32 v38, v38, v63, vcc\n"
"  v_mov_b32 v63, 0x3f800000\n"
"  v_cmp_eq_u32 vcc, 5, v61\n"
"  s_nop 1\n"
"  v_cndmask_b32 v38, v38, v63, vcc\n"
"  v_mov_b32 v39, 0\n"
"  v_mov_b32 v63, 0x3f80\n"
"  v_cmp_eq_u32 vcc, 6, v61\n"
"  s_nop 1\n"
"  v_cndmask_b32 v39, v39, v63, vcc\n"
"  v_mov_b32 v63, 0x3f800000\n"
"  v_cmp_eq_u32 vcc, 7, v61\n"
"  s_nop 1\n"
"  v_cndmask_b32 v39, v39, v63, vcc\n"
"  ds_write_b128 v0, v[36:39] offset:31488\n"
"  s_waitcnt lgkmcnt(0)\n"
"  s_waitcnt vmcnt(28)\n"
"  ds_read_b64_tr_b16 a[128:129], v67 offset:0\n"
"  ds_read_b64_tr_b16 a[130:131], v67 offset:2048\n"
"  ds_read_b64_tr_b16 a[132:133], v67 offset:512\n"
"  ds_read_b64_tr_b16 a[134:135], v67 offset:2560\n"
"  ds_read_b64_tr_b16 a[136:137], v67 offset:32\n"
"  ds_read_b64_tr_b16 a[138:139], v67 offset:2080\n"
"  ds_read_b64_tr_b16 a[140:141], v67 offset:544\n"
"  ds_read_b64_tr_b16 a[142:143], v67 offset:2592\n"
"  s_waitcnt vmcnt(24)\n"
"  ds_read_b64_tr_b16 a[160:161], v67 offset:4096\n"
"  ds_read_b64_tr_b16 a[162:163], v67 offset:6144\n"
"  ds_read_b64_tr_b16 a[164:165], v67 offset:4608\n"
"  ds_read_b64_tr_b16 a[166:167], v67 offset:6656\n"
"  s_waitcnt lgkmcnt(4)\n"
"  s_add_u32 m0, s91, 0\n"
"  s_xor_b32 s68, s92, 2097152\n"
"  buffer_load_dwordx4 v65, s[44:47], s68 offen lds\n"
"  buffer_load_dwordx4 v33, s[44:47], s68 offen offset:1024 lds\n"
"  buffer_load_dwordx4 v34, s[44:47], s68 offen offset:2048 lds\n"
"  buffer_load_dwordx4 v35, s[44:47], s68 offen offset:3072 lds\n"
"  ds_read_b64_tr_b16 a[168:169], v67 offset:4128\n"
"  ds_read_b64_tr_b16 a[170:171], v67 offset:6176\n"
"  ds_read_b64_tr_b16 a[172:173], v67 offset:4640\n"
"  ds_read_b64_tr_b16 a[174:175], v67 offset:6688\n"
"  s_waitcnt vmcnt(24)\n"
"  ds_read_b64_tr_b16 a[144:145], v67 offset:8192\n"
"  ds_read_b64_tr_b16 a[146:147], v67 offset:10240\n"
"  ds_read_b64_tr_b16 a[148:149], v67 offset:8704\n"
"  ds_read_b64_tr_b16 a[150:151], v67 offset:10752\n"
"  s_waitcnt lgkmcnt(4)\n"
"  s_add_u32 m0, s91, 4096\n"
"  s_xor_b32 s68, s92, 3145728\n"
"  buffer_load_dwordx4 v65, s[44:47], s68 offen lds\n"
"  buffer_load_dwordx4 v33, s[44:47], s68 offen offset:1024 lds\n"
"  buffer_load_dwordx4 v34, s[44:47], s68 offen offset:2048 lds\n"
"  buffer_load_dwordx4 v35, s[44:47], s68 offen offset:3072 lds\n"
"  ds_read_b64_tr_b16 a[152:153], v67 offset:8224\n"
"  ds_read_b64_tr_b16 a[154:155], v67 offset:10272\n"
"  ds_read_b64_tr_b16 a[156:157], v67 offset:8736\n"
"  ds_read_b64_tr_b16 a[158:159], v67 offset:10784\n"
"  s_waitcnt vmcnt(24)\n"
"  ds_read_b64_tr_b16 a[176:177], v67 offset:12288\n"
"  ds_read_b64_tr_b16 a[178:179], v67 offset:14336\n"
"  ds_read_b64_tr_b16 a[180:181], v67 offset:12800\n"
"  ds_read_b64_tr_b16 a[182:183], v67 offset:14848\n"
"  s_waitcnt lgkmcnt(4)\n"
"  s_add_u32 m0, s91, 8192\n"
"  s_xor_b32 s68, s92, 2621440\n"
"  buffer_load_dwordx4 v65, s[44:47], s68 offen lds\n"
"  buffer_load_dwordx4 v33, s[44:47], s68 offen offset:1024 lds\n"
"  buffer_load_dwordx4 v34, s[44:47], s68 offen offset:2048 lds\n"
"  buffer_load_dwordx4 v35, s[44:47], s68 offen offset:3072 lds\n"
"  ds_read_b64_tr_b16 a[184:185], v67 offset:12320\n"
"  ds_read_b64_tr_b16 a[186:187], v67 offset:14368\n"
"  ds_read_b64_tr_b16 a[188:189], v67 offset:12832\n"
"  ds_read_b64_tr_b16 a[190:191], v67 offset:14880\n"
"  s_waitcnt vmcnt(24)\n"
"  ds_read_b64_tr_b16 a[192:193], v67 offset:16384\n"
"  ds_read_b64_tr_b16 a[194:195], v67 offset:18432\n"
"  ds_read_b64_tr_b16 a[196:197], v67 offset:16896\n"
"  ds_read_b64_tr_b16 a[198:199], v67 offset:18944\n"
"  s_waitcnt lgkmcnt(4)\n"
"  s_add_u32 m0, s91, 12288\n"
"  s_xor_b32 s68, s92, 3670016\n"
"  buffer_load_dwordx4 v65, s[44:47], s68 offen lds\n"
"  buffer_load_dwordx4 v33, s[44:47], s68 offen offset:1024 lds\n"
"  buffer_load_dwordx4 v34, s[44:47], s68 offen offset:2048 lds\n"
"  buffer_load_dwordx4 v35, s[44:47], s68 offen offset:3072 lds\n"
"  ds_read_b64_tr_b16 a[200:201], v67 offset:16416\n"
"  ds_read_b64_tr_b16 a[202:203], v67 offset:18464\n"
"  ds_read_b64_tr_b16 a[204:205], v67 offset:16928\n"
"  ds_read_b64_tr_b16 a[206:207], v67 offset:18976\n"
"  s_waitcnt vmcnt(24)\n"
"  ds_read_b64_tr_b16 a[224:225], v67 offset:20480\n"
"  ds_read_b64_tr_b16 a[226:227], v67 offset:22528\n"
"  ds_read_b64_tr_b16 a[228:229], v67 offset:20992\n"
"  ds_read_b64_tr_b16 a[230:231], v67 offset:23040\n"
"  s_waitcnt lgkmcnt(4)\n"
"  s_mov_b64 s[44:45], s[24:25]\n"
"  s_add_u32 m0, s91, 16384\n"
"  s_xor_b32 s68, s92, 2097152\n"
"  buffer_load_dwordx4 v65, s[44:47], s68 offen lds\n"
"  buffer_load_dwordx4 v33, s[44:47], s68 offen offset:1024 lds\n"
"  buffer_load_dwordx4 v34, s[44:47], s68 offen offset:2048 lds\n"
"  buffer_load_dwordx4 v35, s[44:47], s68 offen offset:3072 lds\n"
"  ds_read_b64_tr_b16 a[232:233], v67 offset:20512\n"
"  ds_read_b64_tr_b16 a[234:235], v67 offset:22560\n"
"  ds_read_b64_tr_b16 a[236:237], v67 offset:21024\n"
"  ds_read_b64_tr_b16 a[238:239], v67 offset:23072\n"
"  s_waitcnt vmcnt(24)\n"
"  ds_read_b64_tr_b16 a[208:209], v67 offset:24576\n"
"  ds_read_b64_tr_b16 a[210:211], v67 offset:26624\n"
"  ds_read_b64_tr_b16 a[212:213], v67 offset:25088\n"
"  ds_read_b64_tr_b16 a[214:215], v67 offset:27136\n"
"  s_waitcnt lgkmcnt(4)\n"
"  s_add_u32 m0, s91, 20480\n"
"  s_xor_b32 s68, s92, 3145728\n"
"  buffer_load_dwordx4 v65, s[44:47], s68 offen lds\n"
"  buffer_load_dwordx4 v33, s[44:47], s68 offen offset:1024 lds\n"
"  buffer_load_dwordx4 v34, s[44:47], s68 offen offset:2048 lds\n"
"  buffer_load_dwordx4 v35, s[44:47], s68 offen offset:3072 lds\n"
"  ds_read_b64_tr_b16 a[216:217], v67 offset:24608\n"
"  ds_read_b64_tr_b16 a[218:219], v67 offset:26656\n"
"  ds_read_b64_tr_b16 a[220:221], v67 offset:25120\n"
"  ds_read_b64_tr_b16 a[222:223], v67 offset:27168\n"
"  s_waitcnt vmcnt(24)\n"
"  ds_read_b64_tr_b16 a[240:241], v67 offset:28672\n"
"  ds_read_b64_tr_b16 a[242:243], v67 offset:30720\n"
"  ds_read_b64_tr_b16 a[244:245], v67 offset:29184\n"
"  ds_read_b64_tr_b16 a[246:247], v67 offset:31232\n"
"  s_waitcnt lgkmcnt(4)\n"
"  s_add_u32 m0, s91, 24576\n"
"  s_xor_b32 s68, s92, 2621440\n"
"  buffer_load_dwordx4 v65, s[44:47], s68 offen lds\n"
"  buffer_load_dwordx4 v33, s[44:47], s68 offen offset:1024 lds\n"
"  buffer_load_dwordx4 v34, s[44:47], s68 offen offset:2048 lds\n"
"  buffer_load_dwordx4 v35, s[44:47], s68 offen offset:3072 lds\n"
"  ds_read_b64_tr_b16 a[248:249], v67 offset:28704\n"
"  ds_read_b64_tr_b16 a[250:251], v67 offset:30752\n"
"  ds_read_b64_tr_b16 a[252:253], v67 offset:29216\n"
"  ds_read_b64_tr_b16 a[254:255], v67 offset:31264\n"
"  s_waitcnt vmcnt(24)\n"
"  ds_read_b64_tr_b16 v[128:129], v67 offset:0\n"
"  ds_read_b64_tr_b16 v[130:131], v67 offset:2048\n"
"  ds_read_b64_tr_b16 v[132:133], v67 offset:512\n"
"  ds_read_b64_tr_b16 v[134:135], v67 offset:2560\n"
"  s_waitcnt lgkmcnt(4)\n"
"  s_add_u32 m0, s91, 28672\n"
"  s_xor_b32 s68, s92, 3670016\n"
"  buffer_load_dwordx4 v65, s[44:47], s68 offen lds\n"
"  buffer_load_dwordx4 v33, s[44:47], s68 offen offset:1024 lds\n"
"  buffer_load_dwordx4 v34, s[44:47], s68 offen offset:2048 lds\n"
"  buffer_load_dwordx4 v35, s[44:47], s68 offen offset:3072 lds\n"
"  ds_read_b64_tr_b16 v[136:137], v67 offset:32\n"
"  ds_read_b64_tr_b16 v[138:139], v67 offset:2080\n"
"  ds_read_b64_tr_b16 v[140:141], v67 offset:544\n"
"  ds_read_b64_tr_b16 v[142:143], v67 offset:2592\n"
"  s_waitcnt vmcnt(24)\n"
"  ds_read_b64_tr_b16 v[160:161], v67 offset:4096\n"
"  ds_read_b64_tr_b16 v[162:163], v67 offset:6144\n"
"  ds_read_b64_tr_b16 v[164:165], v67 offset:4608\n"
"  ds_read_b64_tr_b16 v[166:167], v67 offset:6656\n"
"  s_waitcnt lgkmcnt(4)\n"
"  s_mov_b64 s[44:45], s[30:31]\n"
"  s_add_u32 m0, s91, 0\n"
"  s_add_u32 s68, s93, 0\n"
"  buffer_load_dwordx4 v66, s[44:47], s68 offen lds\n"
"  buffer_load_dwordx4 v56, s[44:47], s68 offen offset:1024 lds\n"
"  buffer_load_dwordx4 v57, s[44:47], s68 offen offset:2048 lds\n"
"  buffer_load_dwordx4 v58, s[44:47], s68 offen offset:3072 lds\n"
"  ds_read_b64_tr_b16 v[168:169], v67 offset:4128\n"
"  ds_read_b64_tr_b16 v[170:171], v67 offset:6176\n"
"  ds_read_b64_tr_b16 v[172:173], v67 offset:4640\n"
"  ds_read_b64_tr_b16 v[174:175], v67 offset:6688\n"
"  s_waitcnt vmcnt(24)\n"
"  ds_read_b64_tr_b16 v[144:145], v67 offset:8192\n"
"  ds_read_b64_tr_b16 v[146:147], v67 offset:10240\n"
"  ds_read_b64_tr_b16 v[148:149], v67 offset:8704\n"
"  ds_read_b64_tr_b16 v[150:151], v67 offset:10752\n"
"  s_waitcnt lgkmcnt(4)\n"
"  s_add_u32 m0, s91, 4096\n"
"  s_add_u32 s68, s93, 64\n"
"  buffer_load_dwordx4 v66, s[44:47], s68 offen lds\n"
"  buffer_load_dwordx4 v56, s[44:47], s68 offen offset:1024 lds\n"
"  buffer_load_dwordx4 v57, s[44:47], s68 offen offset:2048 lds\n"
"  buffer_load_dwordx4 v58, s[44:47], s68 offen offset:3072 lds\n"
"  ds_read_b64_tr_b16 v[152:153], v67 offset:8224\n"
"  ds_read_b64_tr_b16 v[154:155], v67 offset:10272\n"
"  ds_read_b64_tr_b16 v[156:157], v67 offset:8736\n"
"  ds_read_b64_tr_b16 v[158:159], v67 offset:10784\n"
"  s_waitcnt vmcnt(24)\n"
"  ds_read_b64_tr_b16 v[40:41], v67 offset:12288\n"
"  ds_read_b64_tr_b16 v[42:43], v67 offset:14336\n"
"  ds_read_b64_tr_b16 v[44:45], v67 offset:12800\n"
"  ds_read_b64_tr_b16 v[46:47], v67 offset:14848\n"
"  s_waitcnt lgkmcnt(4)\n"
"  s_add_u32 m0, s91, 8192\n"
"  s_add_u32 s68, s93, 65536\n"
"  buffer_load_dwordx4 v66, s[44:47], s68 offen lds\n"
"  buffer_load_dwordx4 v56, s[44:47], s68 offen offset:1024 lds\n"
"  buffer_load_dwordx4 v57, s[44:47], s68 offen offset:2048 lds\n"
"  buffer_load_dwordx4 v58, s[44:47], s68 offen offset:3072 lds\n"
"  ds_read_b64_tr_b16 v[48:49], v67 offset:12320\n"
"  ds_read_b64_tr_b16 v[50:51], v67 offset:14368\n"
"  ds_read_b64_tr_b16 v[52:53], v67 offset:12832\n"
"  ds_read_b64_tr_b16 v[54:55], v67 offset:14880\n"
"  s_waitcnt vmcnt(24)\n"
"  ds_read_b64_tr_b16 a[32:33], v67 offset:16384\n"
"  ds_read_b64_tr_b16 a[34:35], v67 offset:18432\n"
"  ds_read_b64_tr_b16 a[36:37], v67 offset:16896\n"
"  ds_read_b64_tr_b16 a[38:39], v67 offset:18944\n"
"  s_waitcnt lgkmcnt(4)\n"
"  s_add_u32 m0, s91, 12288\n"
"  s_add_u32 s68, s93, 65600\n"
"  buffer_load_dwordx4 v66, s[44:47], s68 offen lds\n"
"  buffer_load_dwordx4 v56, s[44:47], s68 offen offset:1024 lds\n"
"  buffer_load_dwordx4 v57, s[44:47], s68 offen offset:2048 lds\n"
"  buffer_load_dwordx4 v58, s[44:47], s68 offen offset:3072 lds\n"
"  ds_read_b64_tr_b16 a[40:41], v67 offset:16416\n"
"  ds_read_b64_tr_b16 a[42:43], v67 offset:18464\n"
"  ds_read_b64_tr_b16 a[44:45], v67 offset:16928\n"
"  ds_read_b64_tr_b16 a[46:47], v67 offset:18976\n"
"  s_waitcnt vmcnt(24)\n"
"  ds_read_b64_tr_b16 a[64:65], v67 offset:20480\n"
"  ds_read_b64_tr_b16 a[66:67], v67 offset:22528\n"
"  ds_read_b64_tr_b16 a[68:69], v67 offset:20992\n"
"  ds_read_b64_tr_b16 a[70:71], v67 offset:23040\n"
"  s_waitcnt lgkmcnt(4)\n"
"  s_mov_b64 s[44:45], s[26:27]\n"
"  s_add_u32 m0, s91, 16384\n"
"  s_xor_b32 s68, s92, 0\n"
"  buffer_load_dwordx4 v65, s[44:47], s68 offen lds\n"
"  buffer_load_dwordx4 v33, s[44:47], s68 offen offset:1024 lds\n"
"  buffer_load_dwordx4 v34, s[44:47], s68 offen offset:2048 lds\n"
"  buffer_load_dwordx4 v35, s[44:47], s68 offen offset:3072 lds\n"
"  ds_read_b64_tr_b16 a[72:73], v67 offset:20512\n"
"  ds_read_b64_tr_b16 a[74:75], v67 offset:22560\n"
"  ds_read_b64_tr_b16 a[76:77], v67 offset:21024\n"
"  ds_read_b64_tr_b16 a[78:79], v67 offset:23072\n"
"  s_waitcnt vmcnt(24)\n"
"  ds_read_b64_tr_b16 a[48:49], v67 offset:24576\n"
"  ds_read_b64_tr_b16 a[50:51], v67 offset:26624\n"
"  ds_read_b64_tr_b16 a[52:53], v67 offset:25088\n"
"  ds_read_b64_tr_b16 a[54:55], v67 offset:27136\n"
"  s_waitcnt lgkmcnt(4)\n"
"  s_add_u32 m0, s91, 20480\n"
"  s_xor_b32 s68, s92, 1048576\n"
"  buffer_load_dwordx4 v65, s[44:47], s68 offen lds\n"
"  buffer_load_dwordx4 v33, s[44:47], s68 offen offset:1024 lds\n"
"  buffer_load_dwordx4 v34, s[44:47], s68 offen offset:2048 lds\n"
"  buffer_load_dwordx4 v35, s[44:47], s68 offen offset:3072 lds\n"
"  ds_read_b64_tr_b16 a[56:57], v67 offset:24608\n"
"  ds_read_b64_tr_b16 a[58:59], v67 offset:26656\n"
"  ds_read_b64_tr_b16 a[60:61], v67 offset:25120\n"
"  ds_read_b64_tr_b16 a[62:63], v67 offset:27168\n"
"  s_waitcnt vmcnt(24)\n"
"  ds_read_b64_tr_b16 a[80:81], v67 offset:28672\n"
"  ds_read_b64_tr_b16 a[82:83], v67 offset:30720\n"
"  ds_read_b64_tr_b16 a[84:85], v67 offset:29184\n"
"  ds_read_b64_tr_b16 a[86:87], v67 offset:31232\n"
"  s_waitcnt lgkmcnt(4)\n"
"  s_add_u32 m0, s91, 24576\n"
"  s_xor_b32 s68, s92, 524288\n"
"  buffer_load_dwordx4 v65, s[44:47], s68 offen lds\n"
"  buffer_load_dwordx4 v33, s[44:47], s68 offen offset:1024 lds\n"
"  buffer_load_dwordx4 v34, s[44:47], s68 offen offset:2048 lds\n"
"  buffer_load_dwordx4 v35, s[44:47], s68 offen offset:3072 lds\n"
"  ds_read_b64_tr_b16 a[88:89], v67 offset:28704\n"
"  ds_read_b64_tr_b16 a[90:91], v67 offset:30752\n"
"  ds_read_b64_tr_b16 a[92:93], v67 offset:29216\n"
"  ds_read_b64_tr_b16 a[94:95], v67 offset:31264\n"
"  s_mov_b32 s70, 2162944\n"
"  s_cmp_eq_u32 s63, 3\n"
"  s_cselect_b32 s70, s70, 0x7e000000\n"
"  v_not_b32 v116, s90\n"
"  v_not_b32 v117, s90\n"
"  v_not_b32 v118, s90\n"
"  v_not_b32 v119, s90\n"
"  buffer_load_dwordx4 v[116:119], v0, s[40:43], s70 offen sc1\n"
"  s_waitcnt vmcnt(24)\n"
"  ds_read_b64_tr_b16 v[176:177], v68 offset:0\n"
"  ds_read_b64_tr_b16 v[178:179], v68 offset:128\n"
"  ds_read_b64_tr_b16 v[180:181], v68 offset:2048\n"
"  ds_read_b64_tr_b16 v[182:183], v68 offset:2176\n"
"  s_waitcnt lgkmcnt(4)\n"
"  s_add_u32 m0, s91, 28672\n"
"  s_xor_b32 s68, s92, 1572864\n"
"  buffer_load_dwordx4 v65, s[44:47], s68 offen lds\n"
"  buffer_load_dwordx4 v33, s[44:47], s68 offen offset:1024 lds\n"
"  buffer_load_dwordx4 v34, s[44:47], s68 offen offset:2048 lds\n"
"  buffer_load_dwordx4 v35, s[44:47], s68 offen offset:3072 lds\n"
"  ds_read_b64_tr_b16 v[184:185], v68 offset:32\n"
"  ds_read_b64_tr_b16 v[186:187], v68 offset:160\n"
"  ds_read_b64_tr_b16 v[188:189], v68 offset:2080\n"
"  ds_read_b64_tr_b16 v[190:191], v68 offset:2208\n"
"  s_waitcnt vmcnt(24)\n"
"  ds_read_b64_tr_b16 v[192:193], v68 offset:4096\n"
"  ds_read_b64_tr_b16 v[194:195], v68 offset:4224\n"
"  ds_read_b64_tr_b16 v[196:197], v68 offset:6144\n"
"  ds_read_b64_tr_b16 v[198:199], v68 offset:6272\n"
"  s_waitcnt lgkmcnt(4)\n"
"  s_mov_b64 s[44:45], s[24:25]\n"
"  s_add_u32 m0, s91, 0\n"
"  s_xor_b32 s68, s92, 0\n"
"  buffer_load_dwordx4 v65, s[44:47], s68 offen lds\n"
"  buffer_load_dwordx4 v33, s[44:47], s68 offen offset:1024 lds\n"
"  buffer_load_dwordx4 v34, s[44:47], s68 offen offset:2048 lds\n"
"  buffer_load_dwordx4 v35, s[44:47], s68 offen offset:3072 lds\n"
"  ds_read_b64_tr_b16 v[200:201], v68 offset:4128\n"
"  ds_read_b64_tr_b16 v[202:203], v68 offset:4256\n"
"  ds_read_b64_tr_b16 v[204:205], v68 offset:6176\n"
"  ds_read_b64_tr_b16 v[206:207], v68 offset:6304\n"
"  s_waitcnt vmcnt(24)\n"
"  ds_read_b64_tr_b16 v[208:209], v68 offset:8192\n"
"  ds_read_b64_tr_b16 v[210:211], v68 offset:8320\n"
"  ds_read_b64_tr_b16 v[212:213], v68 offset:10240\n"
"  ds_read_b64_tr_b16 v[214:215], v68 offset:10368\n"
"  s_waitcnt lgkmcnt(4)\n"
"  s_add_u32 m0, s91, 4096\n"
"  s_xor_b32 s68, s92, 1048576\n"
"  buffer_load_dwordx4 v65, s[44:47], s68 offen lds\n"
"  buffer_load_dwordx4 v33, s[44:47], s68 offen offset:1024 lds\n"
"  buffer_load_dwordx4 v34, s[44:47], s68 offen offset:2048 lds\n"
"  buffer_load_dwordx4 v35, s[44:47], s68 offen offset:3072 lds\n"
"  ds_read_b64_tr_b16 v[216:217], v68 offset:8224\n"
"  ds_read_b64_tr_b16 v[218:219], v68 offset:8352\n"
"  ds_read_b64_tr_b16 v[220:221], v68 offset:10272\n"
"  ds_read_b64_tr_b16 v[222:223], v68 offset:10400\n"
"  s_waitcnt vmcnt(24)\n"
"  ds_read_b64_tr_b16 v[224:225], v68 offset:12288\n"
"  ds_read_b64_tr_b16 v[226:227], v68 offset:12416\n"
"  ds_read_b64_tr_b16 v[228:229], v68 offset:14336\n"
"  ds_read_b64_tr_b16 v[230:231], v68 offset:14464\n"
"  s_waitcnt lgkmcnt(4)\n"
"  s_add_u32 m0, s91, 8192\n"
"  s_xor_b32 s68, s92, 524288\n"
"  buffer_load_dwordx4 v65, s[44:47], s68 offen lds\n"
"  buffer_load_dwordx4 v33, s[44:47], s68 offen offset:1024 lds\n"
"  buffer_load_dwordx4 v34, s[44:47], s68 offen offset:2048 lds\n"
"  buffer_load_dwordx4 v35, s[44:47], s68 offen offset:3072 lds\n"
"  ds_read_b64_tr_b16 v[232:233], v68 offset:12320\n"
"  ds_read_b64_tr_b16 v[234:235], v68 offset:12448\n"
"  ds_read_b64_tr_b16 v[236:237], v68 offset:14368\n"
"  ds_read_b64_tr_b16 v[238:239], v68 offset:14496\n"
"  s_waitcnt vmcnt(24)\n"
"  ds_read_b64_tr_b16 v[76:77], v67 offset:16384\n"
"  ds_read_b64_tr_b16 v[78:79], v67 offset:18432\n"
"  ds_read_b64_tr_b16 v[80:81], v67 offset:16896\n"
"  ds_read_b64_tr_b16 v[82:83], v67 offset:18944\n"
"  s_waitcnt lgkmcnt(4)\n"
"  s_add_u32 m0, s91, 12288\n"
"  s_xor_b32 s68, s92, 1572864\n"
"  buffer_load_dwordx4 v65, s[44:47], s68 offen lds\n"
"  buffer_load_dwordx4 v33, s[44:47], s68 offen offset:1024 lds\n"
"  buffer_load_dwordx4 v34, s[44:47], s68 offen offset:2048 lds\n"
"  buffer_load_dwordx4 v35, s[44:47], s68 offen offset:3072 lds\n"
"  ds_read_b64_tr_b16 v[84:85], v67 offset:16416\n"
"  ds_read_b64_tr_b16 v[86:87], v67 offset:18464\n"
"  ds_read_b64_tr_b16 v[88:89], v67 offset:16928\n"
"  ds_read_b64_tr_b16 v[90:91], v67 offset:18976\n"
"  s_waitcnt lgkmcnt(0)\n"
"  ds_write_b128 v16, v[76:79] offset:16384\n"
"  ds_write_b128 v16, v[80:83] offset:17408\n"
"  ds_write_b128 v16, v[84:87] offset:18432\n"
"  ds_write_b128 v16, v[88:91] offset:19456\n"
"  s_waitcnt vmcnt(24)\n"
"  ds_read_b64_tr_b16 a[96:97], v67 offset:20480\n"
"  ds_read_b64_tr_b16 a[98:99], v67 offset:22528\n"
"  ds_read_b64_tr_b16 a[100:101], v67 offset:20992\n"
"  ds_read_b64_tr_b16 a[102:103], v67 offset:23040\n"
"  s_waitcnt lgkmcnt(4)\n"
"  ds_read_b64_tr_b16 a[104:105], v67 offset:20512\n"
"  ds_read_b64_tr_b16 a[106:107], v67 offset:22560\n"
"  ds_read_b64_tr_b16 a[108:109], v67 offset:21024\n"
"  ds_read_b64_tr_b16 a[110:111], v67 offset:23072\n"
"  s_mov_b32 s59, 0\n"
"  s_cmp_lg_u32 s63, 3\n"
"  s_cbranch_scc1 L_dnd%=\n"
"L_dn%=:\n"
"  v_min3_u32 v114, v116, v117, v118\n"
"  v_max3_u32 v115, v116, v117, v118\n"
"  v_min_u32 v114, v114, v119\n"
"  v_max_u32 v115, v115, v119\n"
"  v_xor_b32 v114, s90, v114\n"
"  v_xor_b32 v115, s90, v115\n"
"  v_or_b32 v114, v114, v115\n"
"  v_cmp_ne_u32 vcc, 0, v114\n"
"  s_cbranch_vccz L_dnd%=\n"
"  s_add_u32 s59, s59, 1\n"
"  s_cmp_gt_u32 s59, 80000\n"
"  s_cbranch_scc1 L_dnd%=\n"
"  s_waitcnt vmcnt(0)\n"
"  s_sleep 8\n"
"  buffer_load_dwordx4 v[116:119], v0, s[40:43], s70 offen sc1\n"
"  s_waitcnt vmcnt(0)\n"
"  s_branch L_dn%=\n"
"L_dnd%=:\n"
"  s_barrier\n"
"  s_waitcnt vmcnt(20)\n"
"  ds_read_b64_tr_b16 v[92:93], v67 offset:24576\n"
"  ds_read_b64_tr_b16 v[94:95], v67 offset:26624\n"
"  ds_read_b64_tr_b16 v[96:97], v67 offset:25088\n"
"  ds_read_b64_tr_b16 v[98:99], v67 offset:27136\n"
"  s_waitcnt lgkmcnt(4)\n"
"  ds_read_b64_tr_b16 v[100:101], v67 offset:24608\n"
"  ds_read_b64_tr_b16 v[102:103], v67 offset:26656\n"
"  ds_read_b64_tr_b16 v[104:105], v67 offset:25120\n"
"  ds_read_b64_tr_b16 v[106:107], v67 offset:27168\n"
"  s_waitcnt lgkmcnt(0)\n"
"  ds_write_b128 v16, v[92:95] offset:24576\n"
"  ds_write_b128 v16, v[96:99] offset:25600\n"
"  ds_write_b128 v16, v[100:103] offset:26624\n"
"  ds_write_b128 v16, v[104:107] offset:27648\n"
"  s_waitcnt vmcnt(16)\n"
"  ds_read_b64_tr_b16 a[112:113], v67 offset:28672\n"
"  ds_read_b64_tr_b16 a[114:115], v67 offset:30720\n"
"  ds_read_b64_tr_b16 a[116:117], v67 offset:29184\n"
"  ds_read_b64_tr_b16 a[118:119], v67 offset:31232\n"
"  s_waitcnt lgkmcnt(4)\n"
"  ds_read_b64_tr_b16 a[120:121], v67 offset:28704\n"
"  ds_read_b64_tr_b16 a[122:123], v67 offset:30752\n"
"  ds_read_b64_tr_b16 a[124:125], v67 offset:29216\n"
"  ds_read_b64_tr_b16 a[126:127], v67 offset:31264\n"
"  s_waitcnt vmcnt(12)\n"
"  ds_read_b64_tr_b16 v[76:77], v67 offset:0\n"
"  ds_read_b64_tr_b16 v[78:79], v67 offset:2048\n"
"  ds_read_b64_tr_b16 v[80:81], v67 offset:512\n"
"  ds_read_b64_tr_b16 v[82:83], v67 offset:2560\n"
"  s_waitcnt lgkmcnt(4)\n"
"  ds_read_b64_tr_b16 v[84:85], v67 offset:32\n"
"  ds_read_b64_tr_b16 v[86:87], v67 offset:2080\n"
"  ds_read_b64_tr_b16 v[88:89], v67 offset:544\n"
"  ds_read_b64_tr_b16 v[90:91], v67 offset:2592\n"
"  s_waitcnt lgkmcnt(0)\n"
"  ds_write_b128 v16, v[76:79] offset:0\n"
"  ds_write_b128 v16, v[80:83] offset:1024\n"
"  ds_write_b128 v16, v[84:87] offset:2048\n"
"  ds_write_b128 v16, v[88:91] offset:3072\n"
"  s_mov_b32 s70, 2097152\n"
"  s_cmp_eq_u32 s63, 3\n"
"  s_cselect_b32 s70, s70, 0x7e000000\n"
"  buffer_load_dwordx2 v[108:109], v14, s[40:43], s70 offen sc1\n"
"  s_lshl_b32 s68, s64, 8\n"
"  s_add_u32 s68, s68, 2138112\n"
"  buffer_load_dwordx4 v[110:113], v27, s[40:43], s68 offen sc1\n"
"  s_waitcnt vmcnt(8)\n"
"  ds_read_b64_tr_b16 a[0:1], v67 offset:4096\n"
"  ds_read_b64_tr_b16 a[2:3], v67 offset:6144\n"
"  ds_read_b64_tr_b16 a[4:5], v67 offset:4608\n"
"  ds_read_b64_tr_b16 a[6:7], v67 offset:6656\n"
"  s_waitcnt lgkmcnt(4)\n"
"  ds_read_b64_tr_b16 a[8:9], v67 offset:4128\n"
"  ds_read_b64_tr_b16 a[10:11], v67 offset:6176\n"
"  ds_read_b64_tr_b16 a[12:13], v67 offset:4640\n"
"  ds_read_b64_tr_b16 a[14:15], v67 offset:6688\n"
"  s_waitcnt vmcnt(4)\n"
"  ds_read_b64_tr_b16 v[92:93], v67 offset:8192\n"
"  ds_read_b64_tr_b16 v[94:95], v67 offset:10240\n"
"  ds_read_b64_tr_b16 v[96:97], v67 offset:8704\n"
"  ds_read_b64_tr_b16 v[98:99], v67 offset:10752\n"
"  s_waitcnt lgkmcnt(4)\n"
"  ds_read_b64_tr_b16 v[100:101], v67 offset:8224\n"
"  ds_read_b64_tr_b16 v[102:103], v67 offset:10272\n"
"  ds_read_b64_tr_b16 v[104:105], v67 offset:8736\n"
"  ds_read_b64_tr_b16 v[106:107], v67 offset:10784\n"
"  s_waitcnt lgkmcnt(0)\n"
"  ds_write_b128 v16, v[92:95] offset:8192\n"
"  ds_write_b128 v16, v[96:99] offset:9216\n"
"  ds_write_b128 v16, v[100:103] offset:10240\n"
"  ds_write_b128 v16, v[104:107] offset:11264\n"
"  s_waitcnt vmcnt(0)\n"
"  ds_read_b64_tr_b16 a[16:17], v67 offset:12288\n"
"  ds_read_b64_tr_b16 a[18:19], v67 offset:14336\n"
"  ds_read_b64_tr_b16 a[20:21], v67 offset:12800\n"
"  ds_read_b64_tr_b16 a[22:23], v67 offset:14848\n"
"  s_waitcnt lgkmcnt(4)\n"
"  ds_read_b64_tr_b16 a[24:25], v67 offset:12320\n"
"  ds_read_b64_tr_b16 a[26:27], v67 offset:14368\n"
"  ds_read_b64_tr_b16 a[28:29], v67 offset:12832\n"
"  ds_read_b64_tr_b16 a[30:31], v67 offset:14880\n"
"  s_waitcnt lgkmcnt(0)\n"
"  ds_write_b128 v16, v[40:43] offset:20480\n"
"  ds_write_b128 v16, v[44:47] offset:21504\n"
"  ds_write_b128 v16, v[48:51] offset:22528\n"
"  ds_write_b128 v16, v[52:55] offset:23552\n"
"  s_waitcnt vmcnt(0)\n"
"  s_mov_b32 s59, 0\n"
"  s_cmp_lg_u32 s63, 3\n"
"  s_cbranch_scc1 L_nfe%=\n"
"L_nf%=:\n"
"  v_readfirstlane_b32 s68, v108\n"
"  v_readfirstlane_b32 s69, v109\n"
"  s_add_u32 s59, s59, 1\n"
"  s_cmp_gt_u32 s59, 80000\n"
"  s_cbranch_scc1 L_nfd%=\n"
"  s_nop 0\n"
"  s_lshr_b32 s88, s69, 8\n"
"  s_cmp_eq_u32 s88, 64\n"
"  s_cbranch_scc1 L_nfd%=\n"
"  s_sleep 6\n"
"  buffer_load_dwordx2 v[108:109], v14, s[40:43], s70 offen sc1\n"
"  s_waitcnt vmcnt(0)\n"
"  s_branch L_nf%=\n"
"L_nfd%=:\n"
"  v_mov_b32 v108, s68\n"
"  v_mov_b32 v109, s69\n"
"  ds_write_b64 v27, v[108:109] offset:24576\n"
"  s_waitcnt lgkmcnt(0)\n"
"L_nfe%=:\n"
"  s_barrier\n"
"  ds_read_b64 v[108:109], v27 offset:24576\n"
"  s_waitcnt lgkmcnt(0)\n"
"  v_readfirstlane_b32 s68, v108\n"
"  v_readfirstlane_b32 s69, v109\n"
"  s_nop 3\n"
"  s_and_b32 s69, s69, 0xff\n"
"  s_cmp_gt_u32 s69, 32\n"
"  s_cselect_b32 s70, 8192, 0\n"
"  s_lshr_b32 s69, s68, 16\n"
"  s_and_b32 s68, s68, 0xffff\n"
"  s_cmp_lt_u32 s68, s69\n"
"  s_cselect_b32 s88, 1, 0\n"
"  v_add_u32 v5, s70, v5\n"
"  v_add_u32 v13, s70, v13\n"
"  v_bfe_u32 v17, v0, 1, 6\n"
"  s_lshl_b32 s68, s53, 7\n"
"  s_add_u32 s68, s68, s70\n"
"  s_cmp_eq_u32 s63, 0\n"
"  s_cselect_b32 s68, s68, 0x30000000\n"
"  v_add_u32 v17, s68, v17\n"
"  v_bfe_u32 v75, v0, 7, 1\n"
"  v_lshl_add_u32 v17, v75, 14, v17\n"
"  v_bfe_u32 v75, v0, 8, 1\n"
"  v_lshl_add_u32 v17, v75, 6, v17\n"
"  v_lshrrev_b32 v75, 9, v0\n"
"  v_lshl_add_u32 v17, v75, 30, v17\n"
"  s_mov_b32 s48, 0\n"
"  s_mov_b32 s49, 1572864\n"
"  s_mov_b32 s71, 1605632\n"
"  s_xor_b32 s68, s63, s88\n"
"  s_and_b32 s68, s68, 1\n"
"  s_lshl_b32 s50, s68, 14\n"
"  s_lshr_b32 s68, s64, 3\n"
"  s_xor_b32 s68, s68, s88\n"
"  s_and_b32 s68, s68, 1\n"
"  s_lshl_b32 s68, s68, 14\n"
"  s_xor_b32 s69, s68, 0x4000\n"
"  v_add_u32 v5, s69, v5\n"
"  s_add_u32 s92, s68, s70\n"
"  s_lshr_b32 s68, s64, 4\n"
"  s_lshl_b32 s68, s68, 18\n"
"  s_add_u32 s92, s92, s68\n"
"  s_bfe_u32 s68, s64, 0x20001\n"
"  s_lshl_b32 s68, s68, 15\n"
"  s_add_u32 s92, s92, s68\n"
"  s_lshl_b32 s68, s51, 17\n"
"  s_add_u32 s92, s92, s68\n"
"  s_and_b32 s68, s64, 1\n"
"  s_lshl_b32 s68, s68, 12\n"
"  s_add_u32 s92, s92, s68\n"
"  s_mov_b32 s52, s92\n"
"  s_lshl_b32 s56, s63, 12\n"
"  s_xor_b32 s54, s92, 0x100000\n"
"  s_lshl_b32 s68, s64, 14\n"
"  s_add_u32 s55, s68, 58720256\n"
"  s_lshl_b32 s68, s64, 12\n"
"  s_add_u32 s57, s68, 92274688\n"
"  s_mov_b32 s45, s101\n"
"  s_add_u32 s68, s70, 1835008\n"
"  s_cmp_eq_u32 s63, 1\n"
"  s_cselect_b32 s95, s68, 0x7e000000\n"
"  s_lshl_b32 s68, s64, 8\n"
"  s_add_u32 s94, s95, s68\n"
"  s_lshl_b32 s68, s51, 14\n"
"  s_add_u32 s94, s94, s68\n"
"  s_lshl_b32 s68, s64, 7\n"
"  s_add_u32 s68, s68, 2158592\n"
"  s_lshl_b32 s69, s51, 2\n"
"  s_or_b32 s69, s69, s63\n"
"  s_cmp_eq_u32 s69, 3\n"
"  s_cselect_b32 s93, s68, 0x7e000000\n"
"  s_lshl_b32 s91, s64, 8\n"
"  s_add_u32 s91, s91, 2138112\n"
"  s_mov_b32 s47, 0\n"
"  v_readfirstlane_b32 s68, v110\n"
"  s_nop 3\n"
"  s_cmp_lg_u32 s68, 0\n"
"  s_cbranch_scc1 L_kw0%=d\n"
"  s_mov_b32 s59, 0\n"
"L_kw0%=:\n"
"  buffer_load_dword v18, v27, s[40:43], s91 offen sc1\n"
"  s_waitcnt vmcnt(0)\n"
"  v_readfirstlane_b32 s68, v18\n"
"  s_nop 3\n"
"  s_lshr_b32 s68, s68, s47\n"
"  s_or_b32 s68, s68, s60\n"
"  s_cbranch_scc1 L_kw0%=d\n"
"  s_sleep 2\n"
"  s_add_u32 s59, s59, 1\n"
"  s_cmp_gt_u32 s59, 80000\n"
"  s_cselect_b32 s60, 1, 0\n"
"  s_branch L_kw0%=\n"
"L_kw0%=d:\n"
"  s_add_u32 s91, s91, 4\n"
"  s_lshl_b32 s68, s63, 12\n"
"  v_add_u32 v63, s68, v0\n"
"  buffer_load_dwordx4 v[88:91], v63, s[40:43], s55 offen offset:0 sc1\n"
"  buffer_load_dwordx4 v[92:95], v63, s[40:43], s55 offen offset:1024 sc1\n"
"  buffer_load_dwordx4 v[96:99], v63, s[40:43], s55 offen offset:2048 sc1\n"
"  buffer_load_dwordx4 v[100:103], v63, s[40:43], s55 offen offset:3072 sc1\n"
"  s_waitcnt lgkmcnt(0)\n"
"  s_memtime s[32:33]\n"
"  s_memrealtime s[72:73]\n"
"  s_nop 2\n"
"  s_memrealtime s[74:75]\n"
"  s_nop 2\n"
"  s_memrealtime s[76:77]\n"
"  s_nop 2\n"
"  s_memrealtime s[78:79]\n"
"  s_nop 2\n"
"  s_memrealtime s[80:81]\n"
"  s_nop 2\n"
"  s_memrealtime s[82:83]\n"
"  s_nop 2\n"
"  s_memrealtime s[46:47]\n"
"  s_nop 2\n"
"  s_memrealtime s[88:89]\n"
"  s_waitcnt lgkmcnt(0)\n"
"  s_mov_b32 s68, 0\n"
"  s_cmp_lg_u32 s88, s72\n"
"  s_cselect_b32 s69, s88, s69\n"
"  s_cselect_b32 s68, 112, s68\n"
"  s_cmp_lg_u32 s46, s72\n"
"  s_cselect_b32 s69, s46, s69\n"
"  s_cselect_b32 s68, 96, s68\n"
"  s_cmp_lg_u32 s82, s72\n"
"  s_cselect_b32 s69, s82, s69\n"
"  s_cselect_b32 s68, 80, s68\n"
"  s_cmp_lg_u32 s80, s72\n"
"  s_cselect_b32 s69, s80, s69\n"
"  s_cselect_b32 s68, 64, s68\n"
"  s_cmp_lg_u32 s78, s72\n"
"  s_cselect_b32 s69, s78, s69\n"
"  s_cselect_b32 s68, 48, s68\n"
"  s_cmp_lg_u32 s76, s72\n"
"  s_cselect_b32 s69, s76, s69\n"
"  s_cselect_b32 s68, 32, s68\n"
"  s_cmp_lg_u32 s74, s72\n"
"  s_cselect_b32 s69, s74, s69\n"
"  s_cselect_b32 s68, 16, s68\n"
"  s_cmp_eq_u32 s68, 0\n"
"  s_cbranch_scc1 L_an1%=\n"
"  s_add_u32 s68, s68, s32\n"
"  s_mul_i32 s69, s69, 24\n"
"  s_sub_u32 s72, s68, s35\n"
"  s_sub_u32 s73, s69, s34\n"
"  v_cvt_f32_u32 v120, s72\n"
"  v_cvt_f32_u32 v121, s73\n"
"  v_rcp_f32 v122, v120\n"
"  v_rcp_f32 v123, v121\n"
"  v_mul_f32 v122, v122, v121\n"
"  v_mul_f32 v123, v123, v120\n"
"  v_mul_f32 v122, 0x47800000, v122\n"
"  v_mul_f32 v123, 0x47800000, v123\n"
"  v_cvt_u32_f32 v122, v122\n"
"  v_cvt_u32_f32 v123, v123\n"
"  v_readfirstlane_b32 s36, v122\n"
"  v_readfirstlane_b32 s37, v123\n"
"  s_nop 2\n"
"  s_min_u32 s36, s36, 0x20000\n"
"  s_min_u32 s37, s37, 0x20000\n"
"  s_mov_b32 s35, s68\n"
"  s_mov_b32 s34, s69\n"
"L_an1%=:\n"
"  s_waitcnt vmcnt(3)\n"
"  ds_write_b128 v63, v[88:91] offset:8192\n"
"  s_waitcnt vmcnt(2)\n"
"  ds_write_b128 v63, v[92:95] offset:9216\n"
"  s_waitcnt vmcnt(1)\n"
"  ds_write_b128 v63, v[96:99] offset:10240\n"
"  s_waitcnt vmcnt(0)\n"
"  ds_write_b128 v63, v[100:103] offset:11264\n"
"  s_mov_b32 s47, 0\n"
"  v_readfirstlane_b32 s68, v111\n"
"  s_nop 3\n"
"  s_cmp_lg_u32 s68, 0\n"
"  s_cbranch_scc1 L_kw1%=d\n"
"  s_mov_b32 s59, 0\n"
"L_kw1%=:\n"
"  buffer_load_dword v18, v27, s[40:43], s91 offen sc1\n"
"  s_waitcnt vmcnt(0)\n"
"  v_readfirstlane_b32 s68, v18\n"
"  s_nop 3\n"
"  s_lshr_b32 s68, s68, s47\n"
"  s_or_b32 s68, s68, s60\n"
"  s_cbranch_scc1 L_kw1%=d\n"
"  s_sleep 2\n"
"  s_add_u32 s59, s59, 1\n"
"  s_cmp_gt_u32 s59, 80000\n"
"  s_cselect_b32 s60, 1, 0\n"
"  s_branch L_kw1%=\n"
"L_kw1%=d:\n"
"  s_add_u32 s91, s91, 4\n"
"  s_waitcnt vmcnt(0)\n"
"  s_waitcnt lgkmcnt(0)\n"
"  s_add_u32 s55, s55, 524288\n"
"  s_add_u32 s44, s55, s56\n"
"  s_add_u32 s55, s55, 524288\n"
"  s_mov_b32 s58, 0x7f000000\n"
"  s_sub_u32 s39, s35, s38\n"
"  s_cmp_eq_u32 s63, 1\n"
"  s_cselect_b32 s36, s36, 0\n"
"  s_cselect_b32 s34, s34, 0\n"
"  s_cselect_b32 s94, s94, s93\n"
"  s_cselect_b32 s93, 4, 0\n"
"  s_mov_b32 s59, 0\n"
"  buffer_load_dwordx4 v[32:35], v5, s[40:43], s49 offen sc1\n"
"  buffer_load_dwordx4 v[36:39], v5, s[40:43], s49 offen offset:1024 sc1\n"
"  ds_read_b128 v[48:51], v16 offset:0\n"
"  ds_read_b128 v[52:55], v16 offset:1024\n"
"  ds_read_b128 v[56:59], v16 offset:2048\n"
"  ds_read_b128 v[60:63], v16 offset:3072\n"
"  ds_read_b128 v[88:91], v16 offset:8192\n"
"  ds_read_b128 v[92:95], v16 offset:9216\n"
"  ds_read_b128 v[96:99], v16 offset:10240\n"
"  ds_read_b128 v[100:103], v16 offset:11264\n"
"  ds_read_b128 v[104:107], v16 offset:16384\n"
"  ds_read_b128 v[108:111], v16 offset:17408\n"
"  ds_read_b128 v[112:115], v16 offset:18432\n"
"  ds_read_b128 v[116:119], v16 offset:19456\n"
"  v_mov_b64 v[64:65], 0\n"
"  v_mov_b64 v[66:67], 0\n"
"  v_mov_b64 v[68:69], 0\n"
"  v_mov_b64 v[70:71], 0\n"
"  v_mov_b64 v[72:73], 0\n"
"  v_mov_b64 v[74:75], 0\n"
"  v_mov_b64 v[76:77], 0\n"
"  v_mov_b64 v[78:79], 0\n"
"  v_mov_b64 v[80:81], 0\n"
"  v_mov_b64 v[82:83], 0\n"
"  v_mov_b64 v[84:85], 0\n"
"  v_mov_b64 v[86:87], 0\n"
"  s_waitcnt lgkmcnt(8)\n"
"  ds_read_b128 v[240:243], v16 offset:24576\n"
"  ds_read_b128 v[244:247], v16 offset:25600\n"
"  ds_read_b128 v[248:251], v16 offset:26624\n"
"  ds_read_b128 v[252:255], v16 offset:27648\n"
"  s_branch L_top%=\n"
"  .p2align 6\n"
"  s_nop 0\n"
"  s_nop 0\n"
"  s_nop 0\n"
"  s_nop 0\n"
"  s_nop 0\n"
"  s_nop 0\n"
"  s_nop 0\n"
"  s_nop 0\n"
"  s_nop 0\n"
"  s_nop 0\n"
"  s_nop 0\n"
"  s_nop 0\n"
"  s_nop 0\n"
"L_top0%=:\n"
"  v_mov_b64 v[64:65], 0\n"
"  v_mov_b64 v[66:67], 0\n"
"  v_mov_b64 v[68:69], 0\n"
"  v_mov_b64 v[70:71], 0\n"
"  v_mov_b64 v[72:73], 0\n"
"  v_mov_b64 v[74:75], 0\n"
"  v_mov_b64 v[76:77], 0\n"
"  v_mov_b64 v[78:79], 0\n"
"  v_mov_b64 v[80:81], 0\n"
"  v_mov_b64 v[82:83], 0\n"
"  v_mov_b64 v[84:85], 0\n"
"  v_mov_b64 v[86:87], 0\n"
"  s_add_u32 s48, s48, 1\n"
"  s_mov_b32 s49, s71\n"
"  s_add_u32 s71, s71, 32768\n"
"  s_and_b32 s71, s71, 0xfffbffff\n"
"  s_lshl_b32 s54, s63, 10\n"
"  s_add_u32 s54, s54, s52\n"
"  s_xor_b32 s68, s52, s92\n"
"  s_cmp_eq_u32 s68, 0\n"
"  s_cselect_b32 s69, 0x84000, 0\n"
"  s_cmp_eq_u32 s68, 0x84000\n"
"  s_cselect_b32 s69, 0x100000, s69\n"
"  s_xor_b32 s52, s69, s92\n"
"  s_xor_b32 s50, s50, s68\n"
"  s_xor_b32 s50, s50, s69\n"
"  s_add_u32 s44, s55, s56\n"
"  s_add_u32 s55, s55, 524288\n"
"  s_lshl_b32 s68, s64, 1\n"
"  s_add_u32 s68, s68, s51\n"
"  s_lshl_b32 s68, s68, 12\n"
"  s_add_u32 s68, s68, 24903680\n"
"  s_cmp_eq_u32 s48, 63\n"
"  s_cselect_b32 s44, s68, s44\n"
"  s_add_u32 s94, s94, s93\n"
"  s_add_u32 s95, s95, 4\n"
"  s_lshr_b32 s68, s93, 1\n"
"  s_xor_b32 s68, s68, 2\n"
"  s_add_u32 s34, s34, s68\n"
"  s_sub_u32 s62, s62, 4\n"
"  s_max_u32 s62, s62, s45\n"
"  s_add_u32 s57, s57, 131072\n"
"L_top%=:\n"
"  v_mov_b32 v120, s66\n"
"  v_mov_b32 v121, s66\n"
"  v_mov_b32 v122, s66\n"
"  v_mov_b32 v123, s66\n"
"  s_waitcnt lgkmcnt(0)\n"
"  s_waitcnt vmcnt(0)\n"
"L_p2%=:\n"
"  v_smfmac_f32_16x16x64_bf16 v[64:67], v[32:35], v[48:55], v15\n"
"  ds_write_b128 v1, v[32:35] offset:0\n"
"  v_smfmac_f32_16x16x64_bf16 v[68:71], v[32:35], v[56:63], v15\n"
"  ds_write_b128 v1, v[36:39] offset:1024\n"
"  v_smfmac_f32_16x16x64_bf16 v[64:67], v[36:39], v[88:95], v15\n"
"  v_max3_u32 v19, v32, v33, v34\n"
"  v_max3_u32 v30, v35, v36, v37\n"
"  v_smfmac_f32_16x16x64_bf16 v[68:71], v[36:39], v[96:103], v15\n"
"  v_max3_u32 v19, v19, v38, v39\n"
"  v_max_u32 v19, v19, v30\n"
"  v_smfmac_f32_16x16x64_bf16 v[72:75], v[32:35], v[104:111], v15\n"
"  v_cmp_lt_u32 vcc, s65, v19\n"
"  v_smfmac_f32_16x16x64_bf16 v[76:79], v[32:35], v[112:119], v15\n"
"  s_cbranch_vccnz L_r2%=\n"
"L_ok2%=:\n"
"  v_smfmac_f32_16x16x64_bf16 v[72:75], v[36:39], v[240:247], v15\n"
"  v_smfmac_f32_16x16x64_bf16 v[76:79], v[36:39], v[248:255], v15\n"
"  v_smfmac_f32_16x16x64_bf16 v[80:83], v[32:35], a[192:199], v15\n"
"  s_waitcnt lgkmcnt(0)\n"
"  s_barrier\n"
"  s_memtime s[88:89]\n"
"  ds_read_b128 v[40:43], v2 offset:0\n"
"  ds_read_b128 v[44:47], v2 offset:1024\n"
"  v_smfmac_f32_16x16x64_bf16 v[84:87], v[32:35], a[200:207], v15\n"
"  ds_read_b128 v[48:51], v3 offset:0\n"
"  v_smfmac_f32_16x16x64_bf16 v[80:83], v[36:39], a[208:215], v15\n"
"  ds_read_b128 v[52:55], v3 offset:1024\n"
"  v_smfmac_f32_16x16x64_bf16 v[84:87], v[36:39], a[216:223], v15\n"
"  ds_read_b128 v[56:59], v4 offset:0\n"
"  ds_read_b128 v[60:63], v4 offset:1024\n"
"  s_waitcnt lgkmcnt(5)\n"
"  v_smfmac_f32_16x16x64_bf16 v[64:67], v[40:43], a[0:7], v15\n"
"  v_smfmac_f32_16x16x64_bf16 v[68:71], v[40:43], a[8:15], v15\n"
"  s_waitcnt lgkmcnt(4)\n"
"  v_smfmac_f32_16x16x64_bf16 v[64:67], v[44:47], a[16:23], v15\n"
"  s_waitcnt lgkmcnt(0)\n"
"  s_sub_u32 s68, s88, s39\n"
"  v_smfmac_f32_16x16x64_bf16 v[68:71], v[44:47], a[24:31], v15\n"
"  s_mul_i32 s68, s68, s36\n"
"  s_lshr_b32 s68, s68, 16\n"
"  v_smfmac_f32_16x16x64_bf16 v[64:67], v[48:51], a[32:39], v15\n"
"  s_add_u32 s100, s68, s34\n"
"  s_bitset0_b32 s100, 0\n"
"  v_smfmac_f32_16x16x64_bf16 v[68:71], v[48:51], a[40:47], v15\n"
"  v_mov_b32 v30, s100\n"
"  ds_read_b128 v[88:91], v0 offset:8192\n"
"  v_smfmac_f32_16x16x64_bf16 v[64:67], v[52:55], a[48:55], v15\n"
"  buffer_store_dword v30, v14, s[40:43], s94 offen sc1\n"
"  v_smfmac_f32_16x16x64_bf16 v[68:71], v[52:55], a[56:63], v15\n"
"  ds_read_b128 v[104:107], v0 offset:12288\n"
"  v_smfmac_f32_16x16x64_bf16 v[64:67], v[56:59], a[64:71], v15\n"
"  ds_read_b128 v[92:95], v0 offset:9216\n"
"  v_smfmac_f32_16x16x64_bf16 v[68:71], v[56:59], a[72:79], v15\n"
"  ds_read_b128 v[108:111], v0 offset:13312\n"
"  v_smfmac_f32_16x16x64_bf16 v[64:67], v[60:63], a[80:87], v15\n"
"  ds_read_b128 v[96:99], v0 offset:10240\n"
"  v_smfmac_f32_16x16x64_bf16 v[68:71], v[60:63], a[88:95], v15\n"
"  buffer_store_dwordx4 v[120:123], v0, s[40:43], s54 offen sc1\n"
"  v_smfmac_f32_16x16x64_bf16 v[72:75], v[40:43], a[96:103], v15\n"
"  ds_read_b128 v[112:115], v0 offset:14336\n"
"  s_nop 0\n"
"  v_smfmac_f32_16x16x64_bf16 v[76:79], v[40:43], a[104:111], v15\n"
"  s_nop 0\n"
"  v_add_f32 v64, v64, v66\n"
"  v_add_f32 v65, v65, v67\n"
"  v_smfmac_f32_16x16x64_bf16 v[72:75], v[44:47], a[112:119], v15\n"
"  v_add_f32 v68, v68, v70\n"
"  v_add_f32 v69, v69, v71\n"
"  v_smfmac_f32_16x16x64_bf16 v[76:79], v[44:47], a[120:127], v15\n"
"  v_cvt_pk_bf16_f32 v66, v64, v68\n"
"  v_cvt_pk_bf16_f32 v67, v65, v69\n"
"  v_smfmac_f32_16x16x64_bf16 v[72:75], v[48:51], a[128:135], v15\n"
"  ds_write2_b32 v7, v66, v67 offset0:0 offset1:72\n"
"  v_smfmac_f32_16x16x64_bf16 v[76:79], v[48:51], a[136:143], v15\n"
"  s_xor_b32 s69, s71, 131072\n"
"  buffer_store_dwordx2 v[120:121], v17, s[40:43], s69 offen sc1\n"
"  v_smfmac_f32_16x16x64_bf16 v[72:75], v[52:55], a[144:151], v15\n"
"  buffer_store_dwordx4 v[124:127], v0, s[40:43], s58 offen sc1\n"
"  v_smfmac_f32_16x16x64_bf16 v[76:79], v[52:55], a[152:159], v15\n"
"  ds_read_b128 v[100:103], v0 offset:11264\n"
"  v_smfmac_f32_16x16x64_bf16 v[72:75], v[56:59], a[160:167], v15\n"
"  v_smfmac_f32_16x16x64_bf16 v[76:79], v[56:59], a[168:175], v15\n"
"  ds_read_b128 v[116:119], v0 offset:15360\n"
"  s_waitcnt lgkmcnt(2)\n"
"  s_barrier\n"
"  v_smfmac_f32_16x16x64_bf16 v[72:75], v[60:63], a[176:183], v15\n"
"  ds_read_b128 v[120:123], v9 offset:0\n"
"  v_smfmac_f32_16x16x64_bf16 v[76:79], v[60:63], a[184:191], v15\n"
"  ds_read_b128 v[124:127], v9 offset:64\n"
"  v_smfmac_f32_16x16x64_bf16 v[80:83], v[40:43], a[224:231], v15\n"
"  ds_read_b128 v[64:67], v9 offset:128\n"
"  ds_read_b128 v[68:71], v9 offset:192\n"
"  s_nop 2\n"
"  v_add_f32 v72, v72, v74\n"
"  v_add_f32 v73, v73, v75\n"
"  v_add_f32 v76, v76, v78\n"
"  v_add_f32 v77, v77, v79\n"
"  v_cvt_pk_bf16_f32 v74, v72, v76\n"
"  v_cvt_pk_bf16_f32 v75, v73, v77\n"
"  ds_write2_b32 v8, v74, v75 offset0:0 offset1:72\n"
"  v_smfmac_f32_16x16x64_bf16 v[84:87], v[40:43], a[232:239], v15\n"
"  s_waitcnt lgkmcnt(1)\n"
"  v_mfma_f32_16x16x32_bf16 v[32:35], v[88:91], v[120:123], 0\n"
"  buffer_load_dwordx4 v[240:243], v0, s[40:43], s44 offen offset:0 sc1\n"
"  v_mfma_f32_16x16x32_bf16 v[76:79], v[104:107], v[120:123], 0\n"
"  v_mfma_f32_16x16x32_bf16 v[32:35], v[92:95], v[124:127], v[32:35]\n"
"  buffer_load_dwordx4 v[244:247], v0, s[40:43], s44 offen offset:1024 sc1\n"
"  v_mfma_f32_16x16x32_bf16 v[76:79], v[108:111], v[124:127], v[76:79]\n"
"  s_waitcnt lgkmcnt(0)\n"
"  s_barrier\n"
"  ds_read_b128 v[36:39], v10 offset:0\n"
"  ds_read_b128 v[40:43], v10 offset:64\n"
"  ds_read_b128 v[88:91], v10 offset:128\n"
"  ds_read_b128 v[104:107], v10 offset:192\n"
"  v_mfma_f32_16x16x32_bf16 v[32:35], v[96:99], v[64:67], v[32:35]\n"
"  buffer_load_dwordx4 v[248:251], v0, s[40:43], s44 offen offset:2048 sc1\n"
"  v_mfma_f32_16x16x32_bf16 v[76:79], v[112:115], v[64:67], v[76:79]\n"
"  v_mfma_f32_16x16x32_bf16 v[32:35], v[100:103], v[68:71], v[32:35]\n"
"  buffer_load_dwordx4 v[252:255], v0, s[40:43], s44 offen offset:3072 sc1\n"
"  v_mfma_f32_16x16x32_bf16 v[76:79], v[116:119], v[68:71], v[76:79]\n"
"  s_waitcnt lgkmcnt(2)\n"
"  v_mfma_f32_16x16x32_bf16 v[72:75], v[36:39], v[120:123], 0\n"
"  ds_read_b128 v[96:99], v16 offset:20480\n"
"  v_mfma_f32_16x16x32_bf16 v[72:75], v[40:43], v[124:127], v[72:75]\n"
"  ds_read_b128 v[100:103], v16 offset:21504\n"
"  s_waitcnt lgkmcnt(2)\n"
"  v_mfma_f32_16x16x32_bf16 v[72:75], v[88:91], v[64:67], v[72:75]\n"
"  ds_read_b128 v[92:95], v1 offset:16384\n"
"  v_mfma_f32_16x16x32_bf16 v[72:75], v[104:107], v[68:71], v[72:75]\n"
"  ds_read_b128 v[108:111], v1 offset:17408\n"
"  s_cmp_eq_u32 s48, 63\n"
"  buffer_load_dword v18, v6, s[40:43], s95 offen sc1\n"
"  v_max3_f32 v19, v32, v33, v34\n"
"  v_smfmac_f32_16x16x64_bf16 v[80:83], v[44:47], a[240:247], v15\n"
"  v_max3_f32 v19, v19, v35, v76\n"
"  v_max3_f32 v19, v19, v77, v78\n"
"  v_max_f32 v19, v19, v79\n"
"  v_smfmac_f32_16x16x64_bf16 v[84:87], v[44:47], a[248:255], v15\n"
"  s_cbranch_scc1 L_last%=\n"
"  v_max3_f32 v30, v19, v72, v73\n"
"  v_max3_f32 v31, v19, v72, v73\n"
"  v_smfmac_f32_16x16x64_bf16 v[80:83], v[48:51], v[128:135], v15\n"
"  ds_read_b128 v[116:119], v16 offset:22528\n"
"  v_permlane32_swap_b32 v30, v31\n"
"  v_max_f32 v19, v30, v31\n"
"  v_max_f32 v31, v30, v31\n"
"  v_smfmac_f32_16x16x64_bf16 v[84:87], v[48:51], v[136:143], v15\n"
"  ds_read_b128 v[120:123], v16 offset:23552\n"
"  v_permlane16_swap_b32 v19, v31\n"
"  v_max_f32 v19, v19, v31\n"
"  v_mul_f32 v19, 0xbe0293ee, v19\n"
"  v_fma_f32 v32, v32, s67, v19\n"
"  v_exp_f32 v32, v32\n"
"  v_smfmac_f32_16x16x64_bf16 v[80:83], v[52:55], v[144:151], v15\n"
"  v_fma_f32 v33, v33, s67, v19\n"
"  v_exp_f32 v33, v33\n"
"  v_smfmac_f32_16x16x64_bf16 v[84:87], v[52:55], v[152:159], v15\n"
"  v_fma_f32 v34, v34, s67, v19\n"
"  v_exp_f32 v34, v34\n"
"  v_smfmac_f32_16x16x64_bf16 v[80:83], v[56:59], v[160:167], v15\n"
"  v_fma_f32 v35, v35, s67, v19\n"
"  v_exp_f32 v35, v35\n"
"  v_cvt_pk_bf16_f32 v112, v32, v33\n"
"  v_smfmac_f32_16x16x64_bf16 v[84:87], v[56:59], v[168:175], v15\n"
"  v_fma_f32 v76, v76, s67, v19\n"
"  v_exp_f32 v76, v76\n"
"  v_cvt_pk_bf16_f32 v113, v34, v35\n"
"  s_waitcnt lgkmcnt(0)\n"
"  v_smfmac_f32_16x16x64_bf16 v[80:83], v[60:63], v[96:103], v15\n"
"  v_fma_f32 v77, v77, s67, v19\n"
"  v_exp_f32 v77, v77\n"
"  v_smfmac_f32_16x16x64_bf16 v[84:87], v[60:63], v[116:123], v15\n"
"  v_fma_f32 v78, v78, s67, v19\n"
"  v_exp_f32 v78, v78\n"
"  v_cvt_pk_bf16_f32 v114, v76, v77\n"
"  v_fma_f32 v79, v79, s67, v19\n"
"  v_exp_f32 v79, v79\n"
"  v_fma_f32 v72, v72, s67, v19\n"
"  v_cvt_pk_bf16_f32 v115, v78, v79\n"
"  v_exp_f32 v72, v72\n"
"  v_fma_f32 v73, v73, s67, v19\n"
"  v_mfma_f32_16x16x32_bf16 v[64:67], v[20:23], v[112:115], 0\n"
"  v_exp_f32 v73, v73\n"
"  v_add_f32 v80, v80, v82\n"
"  v_mfma_f32_16x16x32_bf16 v[36:39], v[92:95], v[112:115], 0\n"
"  v_cvt_pk_bf16_f32 v24, v72, v73\n"
"  v_add_f32 v81, v81, v83\n"
"  v_mfma_f32_16x16x32_bf16 v[40:43], v[108:111], v[112:115], 0\n"
"  v_cvt_pk_bf16_f32 v26, v80, v81\n"
"  v_add_f32 v84, v84, v86\n"
"  v_mfma_f32_16x16x16_bf16 v[64:67], v[20:21], v[24:25], v[64:67]\n"
"  v_add_f32 v85, v85, v87\n"
"  v_cvt_pk_bf16_f32 v28, v84, v85\n"
"  v_mfma_f32_16x16x16_bf16 v[36:39], v[26:27], v[24:25], v[36:39]\n"
"  s_nop 0\n"
"  v_mfma_f32_16x16x16_bf16 v[40:43], v[28:29], v[24:25], v[40:43]\n"
"  s_nop 0\n"
"  v_mov_b32 v32, 0\n"
"  v_mov_b32 v33, 0\n"
"  v_rcp_f32 v30, v64\n"
"  v_mov_b32 v34, 0\n"
"  v_mov_b32 v35, 0\n"
"  v_pk_mul_f32 v[36:37], v[36:37], v[30:31] op_sel_hi:[1,0]\n"
"  v_pk_mul_f32 v[38:39], v[38:39], v[30:31] op_sel_hi:[1,0]\n"
"  v_pk_mul_f32 v[40:41], v[40:41], v[30:31] op_sel_hi:[1,0]\n"
"  v_pk_mul_f32 v[42:43], v[42:43], v[30:31] op_sel_hi:[1,0]\n"
"  v_cvt_pk_bf16_f32 v124, v36, v37\n"
"  v_cvt_pk_bf16_f32 v125, v38, v39\n"
"  v_cvt_pk_bf16_f32 v126, v40, v41\n"
"  v_cvt_pk_bf16_f32 v127, v42, v43\n"
"  ds_write_b128 v11, v[124:127]\n"
"  v_mov_b64 v[68:69], 0\n"
"  v_mov_b64 v[70:71], 0\n"
"  v_mov_b64 v[72:73], 0\n"
"  v_mov_b64 v[74:75], 0\n"
"  v_mov_b64 v[76:77], 0\n"
"  v_mov_b64 v[78:79], 0\n"
"  v_mov_b32 v116, v26\n"
"  v_mov_b32 v117, v28\n"
"  v_lshrrev_b32 v118, 2, v1\n"
"  v_lshrrev_b32 v119, 2, v0\n"
"  v_add_u32 v118, v118, v119\n"
"  s_waitcnt lgkmcnt(0)\n"
"  s_barrier\n"
"  ds_read_b128 v[60:63], v12\n"
"  ds_read_b128 v[64:67], v12 offset:128\n"
"  ds_write_b64 v118, v[116:117] offset:26880\n"
"  s_waitcnt lgkmcnt(2)\n"
"  v_smfmac_f32_16x16x64_bf16 v[32:35], v[60:63], v[176:183], v15\n"
"  v_smfmac_f32_16x16x64_bf16 v[68:71], v[60:63], v[184:191], v15\n"
"  s_waitcnt lgkmcnt(1)\n"
"  v_smfmac_f32_16x16x64_bf16 v[32:35], v[64:67], v[208:215], v15\n"
"  v_smfmac_f32_16x16x64_bf16 v[68:71], v[64:67], v[216:223], v15\n"
"  v_smfmac_f32_16x16x64_bf16 v[72:75], v[60:63], v[192:199], v15\n"
"  v_smfmac_f32_16x16x64_bf16 v[76:79], v[60:63], v[200:207], v15\n"
"  s_nop 2\n"
"  v_smfmac_f32_16x16x64_bf16 v[72:75], v[64:67], v[224:231], v15\n"
"  s_nop 0\n"
"  v_add_f32 v32, v32, v34\n"
"  v_add_f32 v33, v33, v35\n"
"  v_smfmac_f32_16x16x64_bf16 v[76:79], v[64:67], v[232:239], v15\n"
"  v_add_f32 v68, v68, v70\n"
"  v_add_f32 v69, v69, v71\n"
"  v_cvt_pk_bf16_f32 v120, v32, v68\n"
"  v_cvt_pk_bf16_f32 v122, v33, v69\n"
"  v_add_f32 v72, v72, v74\n"
"  v_add_f32 v73, v73, v75\n"
"  s_nop 1\n"
"  v_add_f32 v76, v76, v78\n"
"  v_add_f32 v77, v77, v79\n"
"  v_cvt_pk_bf16_f32 v121, v72, v76\n"
"  v_cvt_pk_bf16_f32 v123, v73, v77\n"
"  buffer_store_dwordx4 v[120:123], v13, s[40:43], s50 offen sc1\n"
"  s_cmp_lg_u32 s63, 1\n"
"  s_cbranch_scc1 L_oth%=\n"
"  s_memtime s[32:33]\n"
"  s_waitcnt vmcnt(1)\n"
"L_fl%=:\n"
"  v_subrev_u32 v19, s100, v18\n"
"  v_cmp_eq_u32 vcc, s66, v18\n"
"  s_mov_b32 s101, s88\n"
"  v_max_i32_dpp v19, v19, v19 row_shr:1 row_mask:0xf bank_mask:0xf\n"
"  s_cbranch_vccnz L_rf%=\n"
"L_okf%=:\n"
"  s_memtime s[76:77]\n"
"  v_max_i32_dpp v19, v19, v19 row_shr:2 row_mask:0xf bank_mask:0xf\n"
"  s_getpc_b64 s[78:79]\n"
"L_t1%=p:\n"
"  s_add_u32 s78, s78, L_t1%=e-L_t1%=p\n"
"  v_max_i32_dpp v19, v19, v19 row_shr:4 row_mask:0xf bank_mask:0xf\n"
"  s_addc_u32 s79, s79, 0\n"
"  s_nop 0\n"
"  v_max_i32_dpp v19, v19, v19 row_shr:8 row_mask:0xf bank_mask:0xf\n"
"  s_nop 0\n"
"  v_readlane_b32 s68, v19, 15\n"
"  v_readlane_b32 s69, v19, 31\n"
"  v_readlane_b32 s46, v19, 47\n"
"  v_readlane_b32 s47, v19, 63\n"
"  s_nop 1\n"
"  s_max_i32 s68, s68, s69\n"
"  s_nop 0\n"
"  s_max_i32 s46, s46, s47\n"
"  s_max_i32 s68, s68, s46\n"
"  s_min_i32 s68, s68, 8192\n"
"  s_add_u32 s68, s68, s61\n"
"  s_mul_i32 s68, s68, s37\n"
"  s_lshr_b32 s68, s68, 16\n"
"  s_add_u32 s47, s68, s38\n"
"  s_waitcnt lgkmcnt(0)\n"
"  s_sub_u32 s68, s76, s101\n"
"  s_sub_i32 s68, s47, s68\n"
"  s_cmp_gt_i32 s68, 60\n"
"  s_cbranch_scc1 L_t1%=\n"
"  s_barrier\n"
"L_t1x%=:\n"
"  s_sub_u32 s38, s32, s101\n"
"  s_min_u32 s38, s38, 3000\n"
"  s_waitcnt lgkmcnt(0)\n"
"  s_memtime s[32:33]\n"
"  s_memrealtime s[72:73]\n"
"  s_nop 2\n"
"  s_memrealtime s[74:75]\n"
"  s_nop 2\n"
"  s_memrealtime s[76:77]\n"
"  s_nop 2\n"
"  s_memrealtime s[78:79]\n"
"  s_nop 2\n"
"  s_memrealtime s[80:81]\n"
"  s_nop 2\n"
"  s_memrealtime s[82:83]\n"
"  s_nop 2\n"
"  s_memrealtime s[46:47]\n"
"  s_nop 2\n"
"  s_memrealtime s[88:89]\n"
"  s_waitcnt lgkmcnt(0)\n"
"  ds_read_b128 v[88:91], v16 offset:8192\n"
"  ds_read_b128 v[92:95], v16 offset:9216\n"
"  ds_read_b128 v[96:99], v16 offset:10240\n"
"  ds_read_b128 v[100:103], v16 offset:11264\n"
"  ds_read_b128 v[104:107], v16 offset:16384\n"
"  ds_read_b128 v[108:111], v16 offset:17408\n"
"  ds_read_b128 v[112:115], v16 offset:18432\n"
"  ds_read_b128 v[116:119], v16 offset:19456\n"
"  s_mov_b32 s68, 0\n"
"  s_cmp_lg_u32 s88, s72\n"
"  s_cselect_b32 s69, s88, s69\n"
"  s_cselect_b32 s68, 112, s68\n"
"  s_cmp_lg_u32 s46, s72\n"
"  s_cselect_b32 s69, s46, s69\n"
"  s_cselect_b32 s68, 96, s68\n"
"  s_cmp_lg_u32 s82, s72\n"
"  s_cselect_b32 s69, s82, s69\n"
"  s_cselect_b32 s68, 80, s68\n"
"  s_cmp_lg_u32 s80, s72\n"
"  s_cselect_b32 s69, s80, s69\n"
"  s_cselect_b32 s68, 64, s68\n"
"  s_cmp_lg_u32 s78, s72\n"
"  s_cselect_b32 s69, s78, s69\n"
"  s_cselect_b32 s68, 48, s68\n"
"  s_cmp_lg_u32 s76, s72\n"
"  s_cselect_b32 s69, s76, s69\n"
"  s_cselect_b32 s68, 32, s68\n"
"  s_cmp_lg_u32 s74, s72\n"
"  s_cselect_b32 s69, s74, s69\n"
"  s_cselect_b32 s68, 16, s68\n"
"  s_cmp_eq_u32 s68, 0\n"
"  s_cbranch_scc1 L_an2%=\n"
"  s_add_u32 s68, s68, s32\n"
"  s_mul_i32 s69, s69, 24\n"
"  s_sub_u32 s72, s68, s35\n"
"  s_sub_u32 s73, s69, s34\n"
"  v_cvt_f32_u32 v120, s72\n"
"  v_cvt_f32_u32 v121, s73\n"
"  v_rcp_f32 v122, v120\n"
"  v_rcp_f32 v123, v121\n"
"  v_mul_f32 v122, v122, v121\n"
"  v_mul_f32 v123, v123, v120\n"
"  v_mul_f32 v122, 0x47800000, v122\n"
"  v_mul_f32 v123, 0x47800000, v123\n"
"  v_cvt_u32_f32 v122, v122\n"
"  v_cvt_u32_f32 v123, v123\n"
"  v_readfirstlane_b32 s36, v122\n"
"  v_readfirstlane_b32 s37, v123\n"
"  s_nop 2\n"
"  s_min_u32 s36, s36, 0x20000\n"
"  s_min_u32 s37, s37, 0x20000\n"
"  s_mov_b32 s35, s68\n"
"  s_mov_b32 s34, s69\n"
"L_an2%=:\n"
"  s_mov_b32 s101, s32\n"
"  s_sub_u32 s39, s35, s38\n"
"  s_waitcnt lgkmcnt(0)\n"
"  v_add_u32 v123, s56, v0\n"
"  ds_write_b128 v123, v[240:243] offset:8192\n"
"  ds_write_b128 v123, v[244:247] offset:9216\n"
"  ds_write_b128 v123, v[248:251] offset:10240\n"
"  ds_write_b128 v123, v[252:255] offset:11264\n"
"  ds_read_b128 v[48:51], v16 offset:0\n"
"  ds_read_b128 v[52:55], v16 offset:1024\n"
"  ds_read_b128 v[56:59], v16 offset:2048\n"
"  ds_read_b128 v[60:63], v16 offset:3072\n"
"  s_branch L_an3%=\n"
"L_t1%=x:\n"
"L_t1%=:\n"
"  s_cmp_ge_i32 s68, 768\n"
"  s_cbranch_scc1 L_t1%=s\n"
"  s_sub_i32 s68, s68, 90\n"
"  s_max_i32 s68, s68, 0\n"
"  s_mul_i32 s68, s68, 341\n"
"  s_lshr_b32 s68, s68, 10\n"
"  s_and_b32 s68, s68, -4\n"
"  s_sub_u32 s78, s78, s68\n"
"  s_subb_u32 s79, s79, 0\n"
"  s_setpc_b64 s[78:79]\n"
"  s_nop 2\n"
"  s_nop 2\n"
"  s_nop 2\n"
"  s_nop 2\n"
"  s_nop 2\n"
"  s_nop 2\n"
"  s_nop 2\n"
"  s_nop 2\n"
"  s_nop 2\n"
"  s_nop 2\n"
"  s_nop 2\n"
"  s_nop 2\n"
"  s_nop 2\n"
"  s_nop 2\n"
"  s_nop 2\n"
"  s_nop 2\n"
"  s_nop 2\n"
"  s_nop 2\n"
"  s_nop 2\n"
"  s_nop 2\n"
"  s_nop 2\n"
"  s_nop 2\n"
"  s_nop 2\n"
"  s_nop 2\n"
"  s_nop 2\n"
"  s_nop 2\n"
"  s_nop 2\n"
"  s_nop 2\n"
"  s_nop 2\n"
"  s_nop 2\n"
"  s_nop 2\n"
"  s_nop 2\n"
"  s_nop 2\n"
"  s_nop 2\n"
"  s_nop 2\n"
"  s_nop 2\n"
"  s_nop 2\n"
"  s_nop 2\n"
"  s_nop 2\n"
"  s_nop 2\n"
"  s_nop 2\n"
"  s_nop 2\n"
"  s_nop 2\n"
"  s_nop 2\n"
"  s_nop 2\n"
"  s_nop 2\n"
"  s_nop 2\n"
"  s_nop 2\n"
"  s_nop 2\n"
"  s_nop 2\n"
"  s_nop 2\n"
"  s_nop 2\n"
"  s_nop 2\n"
"  s_nop 2\n"
"  s_nop 2\n"
"  s_nop 2\n"
"  s_nop 2\n"
"  s_nop 2\n"
"  s_nop 2\n"
"  s_nop 2\n"
"  s_nop 2\n"
"  s_nop 2\n"
"  s_nop 2\n"
"  s_nop 2\n"
"L_t1%=e:\n"
"  s_barrier\n"
"  s_branch L_t1x%=\n"
"  .p2align 6\n"
"L_oth%=:\n"
"  s_cmp_lg_u32 s63, 0\n"
"  s_cbranch_scc1 L_ntk%=\n"
"  ds_read_b128 v[64:67], v0 offset:31488\n"
"  s_waitcnt lgkmcnt(1)\n"
"  s_waitcnt vmcnt(1)\n"
"  s_barrier\n"
"  buffer_load_dwordx4 v[88:91], v0, s[40:43], s52 offen offset:0 sc1\n"
"  buffer_load_dwordx4 v[92:95], v0, s[40:43], s52 offen offset:1024 sc1\n"
"  buffer_load_dwordx4 v[96:99], v0, s[40:43], s52 offen offset:2048 sc1\n"
"  buffer_load_dwordx4 v[100:103], v0, s[40:43], s52 offen offset:3072 sc1\n"
"  s_mov_b32 s59, 0\n"
"  v_add_u32 v123, s56, v0\n"
"  ds_write_b128 v123, v[240:243] offset:8192\n"
"  ds_write_b128 v123, v[244:247] offset:9216\n"
"  ds_write_b128 v123, v[248:251] offset:10240\n"
"  ds_write_b128 v123, v[252:255] offset:11264\n"
"  s_waitcnt lgkmcnt(0)\n"
"  s_mov_b32 s49, s71\n"
"  v_xor_b32 v74, s71, v17\n"
"  v_mov_b32 v72, s66\n"
"  v_mov_b32 v73, s66\n"
"  ds_read_b128 v[240:243], v16 offset:24576\n"
"  ds_read_b128 v[244:247], v16 offset:25600\n"
"  ds_read_b128 v[248:251], v16 offset:26624\n"
"  ds_read_b128 v[252:255], v16 offset:27648\n"
"  ds_read_b128 v[48:51], v16 offset:0\n"
"  ds_read_b128 v[52:55], v16 offset:1024\n"
"  ds_read_b128 v[56:59], v16 offset:2048\n"
"  ds_read_b128 v[60:63], v16 offset:3072\n"
"  ds_read_b128 v[104:107], v16 offset:16384\n"
"  ds_read_b128 v[108:111], v16 offset:17408\n"
"  ds_read_b128 v[112:115], v16 offset:18432\n"
"  ds_read_b128 v[116:119], v16 offset:19456\n"
"L_p1%=:\n"
"  s_waitcnt vmcnt(3)\n"
"  v_mfma_f32_16x16x32_bf16 v[68:71], v[64:67], v[88:91], 0\n"
"  v_max3_u32 v19, v88, v89, v90\n"
"  v_max_u32 v19, v19, v91\n"
"  s_waitcnt vmcnt(2)\n"
"  v_mfma_f32_16x16x32_bf16 v[68:71], v[64:67], v[92:95], v[68:71]\n"
"  v_max3_u32 v19, v19, v92, v93\n"
"  v_max3_u32 v19, v19, v94, v95\n"
"  s_waitcnt vmcnt(1)\n"
"  v_mfma_f32_16x16x32_bf16 v[68:71], v[64:67], v[96:99], v[68:71]\n"
"  v_max3_u32 v19, v19, v96, v97\n"
"  v_max3_u32 v19, v19, v98, v99\n"
"  s_waitcnt vmcnt(0)\n"
"  v_mfma_f32_16x16x32_bf16 v[68:71], v[64:67], v[100:103], v[68:71]\n"
"  v_max3_u32 v19, v19, v100, v101\n"
"  v_max3_u32 v19, v19, v102, v103\n"
"  v_cmp_lt_u32 vcc, s65, v19\n"
"  s_cbranch_vccnz L_r1%=\n"
"L_ok1%=:\n"
"  s_mov_b32 exec_hi, 0\n"
"  s_nop 2\n"
"  v_add_f32_dpp v68, v68, v68 row_ror:8 row_mask:0xf bank_mask:0xf\n"
"  v_add_f32_dpp v69, v69, v69 row_ror:8 row_mask:0xf bank_mask:0xf\n"
"  v_add_f32_dpp v70, v70, v70 row_ror:8 row_mask:0xf bank_mask:0xf\n"
"  v_add_f32_dpp v71, v71, v71 row_ror:8 row_mask:0xf bank_mask:0xf\n"
"  v_cvt_pk_bf16_f32 v72, v68, v69\n"
"  v_cvt_pk_bf16_f32 v73, v70, v71\n"
"  s_mov_b32 exec_hi, -1\n"
"  buffer_store_dwordx2 v[72:73], v74, s[40:43], 0 offen sc1\n"
"  s_barrier\n"
"  s_mov_b32 s59, 0\n"
"  buffer_load_dwordx4 v[32:35], v5, s[40:43], s49 offen sc1\n"
"  buffer_load_dwordx4 v[36:39], v5, s[40:43], s49 offen offset:1024 sc1\n"
"  ds_read_b128 v[88:91], v16 offset:8192\n"
"  ds_read_b128 v[92:95], v16 offset:9216\n"
"  ds_read_b128 v[96:99], v16 offset:10240\n"
"  ds_read_b128 v[100:103], v16 offset:11264\n"
"  s_branch L_top0%=\n"
"  .p2align 6\n"
"L_ntk%=:\n"
"  s_waitcnt lgkmcnt(0)\n"
"  s_barrier\n"
"  s_and_b32 s46, s63, 1\n"
"  s_lshl_b32 s68, s51, 1\n"
"  s_or_b32 s46, s46, s68\n"
"  v_lshrrev_b32 v120, 8, v0\n"
"  v_mul_u32_u24 v120, 32, v120\n"
"  v_add_u32 v120, v120, v0\n"
"  s_mul_i32 s68, s46, 1152\n"
"  s_add_u32 s68, s68, 2304\n"
"  s_lshl_b32 s69, s46, 10\n"
"  s_sub_u32 s58, s69, 2048\n"
"  s_cmp_lt_u32 s46, 2\n"
"  s_cselect_b32 s68, s68, s58\n"
"  s_cselect_b64 vcc, -1, 0\n"
"  v_cndmask_b32 v120, v0, v120, vcc\n"
"  v_add_u32 v120, s68, v120\n"
"  ds_read_b128 v[124:127], v120 offset:26880\n"
"  s_add_u32 s58, s69, s57\n"
"  s_cmp_ge_u32 s63, 2\n"
"  s_cselect_b32 s58, s58, 0x7f000000\n"
"  s_cmp_lt_u32 s48, 62\n"
"  s_cbranch_scc1 L_ep%=\n"
"  s_waitcnt vmcnt(1)\n"
"  s_waitcnt lgkmcnt(0)\n"
"  buffer_store_dwordx4 v[124:127], v0, s[40:43], s58 offen sc1\n"
"  s_mov_b32 s58, 0x7f000000\n"
"  s_add_u32 s68, s34, 2\n"
"  v_mov_b32 v30, s68\n"
"  buffer_store_dword v30, v14, s[40:43], s94 offen sc1\n"
"L_ep%=:\n"
"  s_waitcnt lgkmcnt(0)\n"
"  ds_read_b128 v[48:51], v16 offset:0\n"
"  ds_read_b128 v[52:55], v16 offset:1024\n"
"  ds_read_b128 v[56:59], v16 offset:2048\n"
"  ds_read_b128 v[60:63], v16 offset:3072\n"
"  ds_read_b128 v[88:91], v16 offset:8192\n"
"  ds_read_b128 v[92:95], v16 offset:9216\n"
"  ds_read_b128 v[96:99], v16 offset:10240\n"
"  ds_read_b128 v[100:103], v16 offset:11264\n"
"  ds_read_b128 v[104:107], v16 offset:16384\n"
"  ds_read_b128 v[108:111], v16 offset:17408\n"
"  ds_read_b128 v[112:115], v16 offset:18432\n"
"  ds_read_b128 v[116:119], v16 offset:19456\n"
"  buffer_load_dword v18, v27, s[40:43], s91 offen sc1\n"
"  s_waitcnt lgkmcnt(0)\n"
"  s_cmp_gt_u32 s48, 61\n"
"  s_cbranch_scc1 L_kv62%=\n"
"  s_waitcnt vmcnt(2)\n"
"L_kv62%=:\n"
"  v_add_u32 v123, s56, v0\n"
"  ds_write_b128 v123, v[240:243] offset:8192\n"
"  ds_write_b128 v123, v[244:247] offset:9216\n"
"  ds_write_b128 v123, v[248:251] offset:10240\n"
"  ds_write_b128 v123, v[252:255] offset:11264\n"
"  s_cbranch_scc1 L_kw2%=d\n"
"  s_cmp_eq_u32 s48, 61\n"
"  s_cselect_b32 s47, 1, 0\n"
"  s_waitcnt vmcnt(0)\n"
"  v_readfirstlane_b32 s68, v18\n"
"  s_nop 3\n"
"  s_lshr_b32 s68, s68, s47\n"
"  s_cbranch_scc1 L_kw2%=d\n"
"  s_mov_b32 s59, 0\n"
"L_kw2%=:\n"
"  buffer_load_dword v18, v27, s[40:43], s91 offen sc1\n"
"  s_waitcnt vmcnt(0)\n"
"  v_readfirstlane_b32 s68, v18\n"
"  s_nop 3\n"
"  s_lshr_b32 s68, s68, s47\n"
"  s_or_b32 s68, s68, s60\n"
"  s_cbranch_scc1 L_kw2%=d\n"
"  s_sleep 2\n"
"  s_add_u32 s59, s59, 1\n"
"  s_cmp_gt_u32 s59, 80000\n"
"  s_cselect_b32 s60, 1, 0\n"
"  s_branch L_kw2%=\n"
"L_kw2%=d:\n"
"  s_add_u32 s91, s91, 4\n"
"L_an3%=:\n"
"  s_waitcnt lgkmcnt(0)\n"
"  s_mov_b32 s49, s71\n"
"  s_cmp_lg_u32 s63, 1\n"
"  s_cbranch_scc1 L_k2d%=\n"
"  s_memtime s[88:89]\n"
"  s_getpc_b64 s[78:79]\n"
"L_k2b%=p:\n"
"  s_add_u32 s78, s78, L_k2b%=e-L_k2b%=p\n"
"  s_addc_u32 s79, s79, 0\n"
"  s_waitcnt lgkmcnt(0)\n"
"  s_sub_u32 s68, s88, s101\n"
"  s_sub_i32 s68, s62, s68\n"
"L_k2b%=:\n"
"  s_cmp_ge_i32 s68, 768\n"
"  s_cbranch_scc1 L_k2b%=s\n"
"  s_max_i32 s68, s68, 0\n"
"  s_mul_i32 s68, s68, 341\n"
"  s_lshr_b32 s68, s68, 10\n"
"  s_and_b32 s68, s68, -4\n"
"  s_sub_u32 s78, s78, s68\n"
"  s_subb_u32 s79, s79, 0\n"
"  s_setpc_b64 s[78:79]\n"
"  s_nop 2\n"
"  s_nop 2\n"
"  s_nop 2\n"
"  s_nop 2\n"
"  s_nop 2\n"
"  s_nop 2\n"
"  s_nop 2\n"
"  s_nop 2\n"
"  s_nop 2\n"
"  s_nop 2\n"
"  s_nop 2\n"
"  s_nop 2\n"
"  s_nop 2\n"
"  s_nop 2\n"
"  s_nop 2\n"
"  s_nop 2\n"
"  s_nop 2\n"
"  s_nop 2\n"
"  s_nop 2\n"
"  s_nop 2\n"
"  s_nop 2\n"
"  s_nop 2\n"
"  s_nop 2\n"
"  s_nop 2\n"
"  s_nop 2\n"
"  s_nop 2\n"
"  s_nop 2\n"
"  s_nop 2\n"
"  s_nop 2\n"
"  s_nop 2\n"
"  s_nop 2\n"
"  s_nop 2\n"
"  s_nop 2\n"
"  s_nop 2\n"
"  s_nop 2\n"
"  s_nop 2\n"
"  s_nop 2\n"
"  s_nop 2\n"
"  s_nop 2\n"
"  s_nop 2\n"
"  s_nop 2\n"
"  s_nop 2\n"
"  s_nop 2\n"
"  s_nop 2\n"
"  s_nop 2\n"
"  s_nop 2\n"
"  s_nop 2\n"
"  s_nop 2\n"
"  s_nop 2\n"
"  s_nop 2\n"
"  s_nop 2\n"
"  s_nop 2\n"
"  s_nop 2\n"
"  s_nop 2\n"
"  s_nop 2\n"
"  s_nop 2\n"
"  s_nop 2\n"
"  s_nop 2\n"
"  s_nop 2\n"
"  s_nop 2\n"
"  s_nop 2\n"
"  s_nop 2\n"
"  s_nop 2\n"
"  s_nop 2\n"
"L_k2b%=e:\n"
"L_k2d%=:\n"
"  s_barrier\n"
"  s_mov_b32 s59, 0\n"
"  buffer_load_dwordx4 v[32:35], v5, s[40:43], s49 offen sc1\n"
"  buffer_load_dwordx4 v[36:39], v5, s[40:43], s49 offen offset:1024 sc1\n"
"  ds_read_b128 v[240:243], v16 offset:24576\n"
"  ds_read_b128 v[244:247], v16 offset:25600\n"
"  ds_read_b128 v[248:251], v16 offset:26624\n"
"  ds_read_b128 v[252:255], v16 offset:27648\n"
"  s_branch L_top0%=\n"
"L_t1%=s:\n"
"  s_sleep 8\n"
"  s_sub_i32 s68, s68, 580\n"
"  s_branch L_t1%=\n"
"L_k2b%=s:\n"
"  s_sleep 8\n"
"  s_sub_i32 s68, s68, 580\n"
"  s_branch L_k2b%=\n"
"L_r2%=:\n"
"  s_add_u32 s86, s86, 1\n"
"  s_add_u32 s59, s59, 1\n"
"  s_and_b32 s68, s59, 31\n"
"  s_cbranch_scc1 L_r2%=f\n"
"  s_nop 7\n"
"  s_mov_b64 s[46:47], 0\n"
"  v_cmp_eq_u32 vcc, s66, v32\n"
"  s_nop 3\n"
"  s_or_b64 s[46:47], s[46:47], vcc\n"
"  v_cmp_eq_u32 vcc, s66, v33\n"
"  s_nop 3\n"
"  s_or_b64 s[46:47], s[46:47], vcc\n"
"  v_cmp_eq_u32 vcc, s66, v34\n"
"  s_nop 3\n"
"  s_or_b64 s[46:47], s[46:47], vcc\n"
"  v_cmp_eq_u32 vcc, s66, v35\n"
"  s_nop 3\n"
"  s_or_b64 s[46:47], s[46:47], vcc\n"
"  v_cmp_eq_u32 vcc, s66, v36\n"
"  s_nop 3\n"
"  s_or_b64 s[46:47], s[46:47], vcc\n"
"  v_cmp_eq_u32 vcc, s66, v37\n"
"  s_nop 3\n"
"  s_or_b64 s[46:47], s[46:47], vcc\n"
"  v_cmp_eq_u32 vcc, s66, v38\n"
"  s_nop 3\n"
"  s_or_b64 s[46:47], s[46:47], vcc\n"
"  v_cmp_eq_u32 vcc, s66, v39\n"
"  s_nop 3\n"
"  s_or_b64 s[46:47], s[46:47], vcc\n"
"  s_cmp_eq_u64 s[46:47], 0\n"
"  s_cbranch_scc1 L_ok2%=\n"
"  s_cmp_gt_u32 s59, 20000\n"
"  s_cselect_b32 s68, 1, 0\n"
"  s_or_b32 s60, s60, s68\n"
"  s_cbranch_scc1 L_ok2%=\n"
"L_r2%=f:\n"
"  s_cmp_eq_u32 s59, 1\n"
"  s_cselect_b32 s68, 0, 0\n"
"  s_add_u32 s62, s62, s68\n"
"  s_min_u32 s62, s62, 1500\n"
"  s_waitcnt lgkmcnt(0)\n"
"  buffer_load_dwordx4 v[32:35], v5, s[40:43], s49 offen sc1\n"
"  buffer_load_dwordx4 v[36:39], v5, s[40:43], s49 offen offset:1024 sc1\n"
"  s_nop 7\n"
"  v_mov_b64 v[64:65], 0\n"
"  v_mov_b64 v[66:67], 0\n"
"  v_mov_b64 v[68:69], 0\n"
"  v_mov_b64 v[70:71], 0\n"
"  v_mov_b64 v[72:73], 0\n"
"  v_mov_b64 v[74:75], 0\n"
"  v_mov_b64 v[76:77], 0\n"
"  v_mov_b64 v[78:79], 0\n"
"  s_waitcnt vmcnt(0)\n"
"  s_branch L_p2%=\n"
"L_r1%=:\n"
"  s_add_u32 s85, s85, 1\n"
"  s_add_u32 s59, s59, 1\n"
"  s_and_b32 s68, s59, 31\n"
"  s_cbranch_scc1 L_r1%=f\n"
"  s_nop 7\n"
"  s_mov_b64 s[46:47], 0\n"
"  v_cmp_eq_u32 vcc, s66, v88\n"
"  s_nop 3\n"
"  s_or_b64 s[46:47], s[46:47], vcc\n"
"  v_cmp_eq_u32 vcc, s66, v89\n"
"  s_nop 3\n"
"  s_or_b64 s[46:47], s[46:47], vcc\n"
"  v_cmp_eq_u32 vcc, s66, v90\n"
"  s_nop 3\n"
"  s_or_b64 s[46:47], s[46:47], vcc\n"
"  v_cmp_eq_u32 vcc, s66, v91\n"
"  s_nop 3\n"
"  s_or_b64 s[46:47], s[46:47], vcc\n"
"  v_cmp_eq_u32 vcc, s66, v92\n"
"  s_nop 3\n"
"  s_or_b64 s[46:47], s[46:47], vcc\n"
"  v_cmp_eq_u32 vcc, s66, v93\n"
"  s_nop 3\n"
"  s_or_b64 s[46:47], s[46:47], vcc\n"
"  v_cmp_eq_u32 vcc, s66, v94\n"
"  s_nop 3\n"
"  s_or_b64 s[46:47], s[46:47], vcc\n"
"  v_cmp_eq_u32 vcc, s66, v95\n"
"  s_nop 3\n"
"  s_or_b64 s[46:47], s[46:47], vcc\n"
"  v_cmp_eq_u32 vcc, s66, v96\n"
"  s_nop 3\n"
"  s_or_b64 s[46:47], s[46:47], vcc\n"
"  v_cmp_eq_u32 vcc, s66, v97\n"
"  s_nop 3\n"
"  s_or_b64 s[46:47], s[46:47], vcc\n"
"  v_cmp_eq_u32 vcc, s66, v98\n"
"  s_nop 3\n"
"  s_or_b64 s[46:47], s[46:47], vcc\n"
"  v_cmp_eq_u32 vcc, s66, v99\n"
"  s_nop 3\n"
"  s_or_b64 s[46:47], s[46:47], vcc\n"
"  v_cmp_eq_u32 vcc, s66, v100\n"
"  s_nop 3\n"
"  s_or_b64 s[46:47], s[46:47], vcc\n"
"  v_cmp_eq_u32 vcc, s66, v101\n"
"  s_nop 3\n"
"  s_or_b64 s[46:47], s[46:47], vcc\n"
"  v_cmp_eq_u32 vcc, s66, v102\n"
"  s_nop 3\n"
"  s_or_b64 s[46:47], s[46:47], vcc\n"
"  v_cmp_eq_u32 vcc, s66, v103\n"
"  s_nop 3\n"
"  s_or_b64 s[46:47], s[46:47], vcc\n"
"  s_cmp_eq_u64 s[46:47], 0\n"
"  s_cbranch_scc1 L_ok1%=\n"
"  s_cmp_gt_u32 s59, 20000\n"
"  s_cselect_b32 s68, 1, 0\n"
"  s_or_b32 s60, s60, s68\n"
"  s_cbranch_scc1 L_ok1%=\n"
"L_r1%=f:\n"
"  buffer_load_dwordx4 v[88:91], v0, s[40:43], s52 offen offset:0 sc1\n"
"  buffer_load_dwordx4 v[92:95], v0, s[40:43], s52 offen offset:1024 sc1\n"
"  buffer_load_dwordx4 v[96:99], v0, s[40:43], s52 offen offset:2048 sc1\n"
"  buffer_load_dwordx4 v[100:103], v0, s[40:43], s52 offen offset:3072 sc1\n"
"  s_nop 3\n"
"  s_branch L_p1%=\n"
"L_rf%=:\n"
"  s_add_u32 s87, s87, 1\n"
"  s_add_u32 s59, s59, 1\n"
"  s_cmp_gt_u32 s59, 64\n"
"  s_cbranch_scc1 L_okf%=\n"
"  buffer_load_dword v18, v6, s[40:43], s95 offen sc1\n"
"  s_waitcnt vmcnt(0)\n"
"  s_branch L_fl%=\n"
"L_last%=:\n"
"  ds_read_b128 v[116:119], v16 offset:22528\n"
"  ds_read_b128 v[120:123], v16 offset:23552\n"
"  s_waitcnt lgkmcnt(0)\n"
"  v_smfmac_f32_16x16x64_bf16 v[80:83], v[48:51], v[128:135], v15\n"
"  v_smfmac_f32_16x16x64_bf16 v[84:87], v[48:51], v[136:143], v15\n"
"  v_smfmac_f32_16x16x64_bf16 v[80:83], v[52:55], v[144:151], v15\n"
"  v_smfmac_f32_16x16x64_bf16 v[84:87], v[52:55], v[152:159], v15\n"
"  v_smfmac_f32_16x16x64_bf16 v[80:83], v[56:59], v[160:167], v15\n"
"  v_smfmac_f32_16x16x64_bf16 v[84:87], v[56:59], v[168:175], v15\n"
"  v_smfmac_f32_16x16x64_bf16 v[80:83], v[60:63], v[96:103], v15\n"
"  v_smfmac_f32_16x16x64_bf16 v[84:87], v[60:63], v[116:123], v15\n"
"  s_nop 6\n"
"  v_add_f32 v80, v80, v82\n"
"  v_add_f32 v81, v81, v83\n"
"  v_add_f32 v84, v84, v86\n"
"  v_add_f32 v85, v85, v87\n"
"  v_cvt_pk_bf16_f32 v26, v80, v81\n"
"  v_cvt_pk_bf16_f32 v28, v84, v85\n"
"  ds_read_b128 v[32:35], v0 offset:8192\n"
"  ds_read_b128 v[36:39], v0 offset:9216\n"
"  ds_read_b128 v[40:43], v0 offset:10240\n"
"  ds_read_b128 v[44:47], v0 offset:11264\n"
"  ds_read_b128 v[48:51], v0 offset:12288\n"
"  ds_read_b128 v[52:55], v0 offset:13312\n"
"  ds_read_b128 v[56:59], v0 offset:14336\n"
"  ds_read_b128 v[60:63], v0 offset:15360\n"
"  ds_read_b128 v[96:99], v10 offset:0\n"
"  ds_read_b128 v[100:103], v10 offset:64\n"
"  ds_read_b128 v[112:115], v10 offset:128\n"
"  ds_read_b128 v[116:119], v10 offset:192\n"
"  v_bfe_u32 v88, v0, 4, 4\n"
"  v_lshrrev_b32 v89, 8, v0\n"
"  v_lshlrev_b32 v89, 2, v89\n"
"  v_sub_u32 v88, v88, v89\n"
"  s_lshl_b32 s68, s51, 4\n"
"  v_add_u32 v88, s68, v88\n"
"  v_mov_b32 v89, 0xff800000\n"
"  s_waitcnt vmcnt(0)\n"
"  s_waitcnt lgkmcnt(11)\n"
"  v_mfma_f32_16x16x32_bf16 v[64:67], v[32:35], v[240:243], 0\n"
"  s_waitcnt lgkmcnt(7)\n"
"  v_mfma_f32_16x16x32_bf16 v[68:71], v[48:51], v[240:243], 0\n"
"  s_waitcnt lgkmcnt(3)\n"
"  v_mfma_f32_16x16x32_bf16 v[72:75], v[96:99], v[240:243], 0\n"
"  v_mfma_f32_16x16x32_bf16 v[64:67], v[36:39], v[244:247], v[64:67]\n"
"  v_mfma_f32_16x16x32_bf16 v[68:71], v[52:55], v[244:247], v[68:71]\n"
"  s_waitcnt lgkmcnt(2)\n"
"  v_mfma_f32_16x16x32_bf16 v[72:75], v[100:103], v[244:247], v[72:75]\n"
"  v_mfma_f32_16x16x32_bf16 v[64:67], v[40:43], v[248:251], v[64:67]\n"
"  v_mfma_f32_16x16x32_bf16 v[68:71], v[56:59], v[248:251], v[68:71]\n"
"  s_waitcnt lgkmcnt(1)\n"
"  v_mfma_f32_16x16x32_bf16 v[72:75], v[112:115], v[248:251], v[72:75]\n"
"  v_mfma_f32_16x16x32_bf16 v[64:67], v[44:47], v[252:255], v[64:67]\n"
"  v_mfma_f32_16x16x32_bf16 v[68:71], v[60:63], v[252:255], v[68:71]\n"
"  s_waitcnt lgkmcnt(0)\n"
"  v_mfma_f32_16x16x32_bf16 v[72:75], v[116:119], v[252:255], v[72:75]\n"
"  v_cmp_le_i32 vcc, 0, v88\n"
"  s_nop 3\n"
"  v_cndmask_b32 v64, v89, v64, vcc\n"
"  v_cmp_le_i32 vcc, 1, v88\n"
"  s_nop 1\n"
"  v_cndmask_b32 v65, v89, v65, vcc\n"
"  v_cmp_le_i32 vcc, 2, v88\n"
"  s_nop 1\n"
"  v_cndmask_b32 v66, v89, v66, vcc\n"
"  v_cmp_le_i32 vcc, 3, v88\n"
"  s_nop 1\n"
"  v_cndmask_b32 v67, v89, v67, vcc\n"
"  v_cmp_le_i32 vcc, 16, v88\n"
"  s_nop 1\n"
"  v_cndmask_b32 v68, v89, v68, vcc\n"
"  v_cmp_le_i32 vcc, 17, v88\n"
"  s_nop 1\n"
"  v_cndmask_b32 v69, v89, v69, vcc\n"
"  v_cmp_le_i32 vcc, 18, v88\n"
"  s_nop 1\n"
"  v_cndmask_b32 v70, v89, v70, vcc\n"
"  v_cmp_le_i32 vcc, 19, v88\n"
"  s_nop 1\n"
"  v_cndmask_b32 v71, v89, v71, vcc\n"
"  v_max3_f32 v19, v64, v65, v66\n"
"  v_max3_f32 v19, v19, v67, v68\n"
"  v_max3_f32 v19, v19, v69, v70\n"
"  v_max_f32 v19, v19, v71\n"
"  v_max3_f32 v30, v19, v72, v73\n"
"  v_max3_f32 v31, v19, v72, v73\n"
"  s_nop 1\n"
"  v_permlane32_swap_b32 v30, v31\n"
"  v_max_f32 v19, v30, v31\n"
"  v_max_f32 v31, v30, v31\n"
"  s_nop 1\n"
"  v_permlane16_swap_b32 v19, v31\n"
"  v_max_f32 v19, v19, v31\n"
"  v_mul_f32 v19, 0xbe0293ee, v19\n"
"  v_fma_f32 v64, v64, s67, v19\n"
"  v_exp_f32 v64, v64\n"
"  v_fma_f32 v65, v65, s67, v19\n"
"  v_exp_f32 v65, v65\n"
"  v_fma_f32 v66, v66, s67, v19\n"
"  v_exp_f32 v66, v66\n"
"  v_fma_f32 v67, v67, s67, v19\n"
"  v_exp_f32 v67, v67\n"
"  v_fma_f32 v68, v68, s67, v19\n"
"  v_exp_f32 v68, v68\n"
"  v_fma_f32 v69, v69, s67, v19\n"
"  v_exp_f32 v69, v69\n"
"  v_fma_f32 v70, v70, s67, v19\n"
"  v_exp_f32 v70, v70\n"
"  v_fma_f32 v71, v71, s67, v19\n"
"  v_exp_f32 v71, v71\n"
"  v_fma_f32 v72, v72, s67, v19\n"
"  v_exp_f32 v72, v72\n"
"  v_fma_f32 v73, v73, s67, v19\n"
"  v_exp_f32 v73, v73\n"
"  v_cvt_pk_bf16_f32 v76, v64, v65\n"
"  v_cvt_pk_bf16_f32 v77, v66, v67\n"
"  v_cvt_pk_bf16_f32 v78, v68, v69\n"
"  v_cvt_pk_bf16_f32 v79, v70, v71\n"
"  v_cvt_pk_bf16_f32 v24, v72, v73\n"
"  s_nop 0\n"
"  v_mfma_f32_16x16x32_bf16 v[32:35], v[20:23], v[76:79], 0\n"
"  v_mfma_f32_16x16x32_bf16 v[36:39], v[92:95], v[76:79], 0\n"
"  v_mfma_f32_16x16x32_bf16 v[40:43], v[108:111], v[76:79], 0\n"
"  s_nop 3\n"
"  v_mfma_f32_16x16x16_bf16 v[32:35], v[20:21], v[24:25], v[32:35]\n"
"  v_mfma_f32_16x16x16_bf16 v[36:39], v[26:27], v[24:25], v[36:39]\n"
"  v_mfma_f32_16x16x16_bf16 v[40:43], v[28:29], v[24:25], v[40:43]\n"
"  s_lshl_b32 s68, s51, 17\n"
"  s_lshl_b32 s69, s64, 12\n"
"  s_add_u32 s68, s68, s69\n"
"  s_lshl_b32 s69, s63, 10\n"
"  s_add_u32 s68, s68, s69\n"
"  s_add_u32 s68, s68, 117178368\n"
"  v_rcp_f32 v30, v32\n"
"  s_nop 0\n"
"  v_mul_f32 v36, v36, v30\n"
"  v_mul_f32 v37, v37, v30\n"
"  v_mul_f32 v38, v38, v30\n"
"  v_mul_f32 v39, v39, v30\n"
"  v_mul_f32 v40, v40, v30\n"
"  v_mul_f32 v41, v41, v30\n"
"  v_mul_f32 v42, v42, v30\n"
"  v_mul_f32 v43, v43, v30\n"
"  v_cvt_pk_bf16_f32 v44, v36, v37\n"
"  v_cvt_pk_bf16_f32 v45, v38, v39\n"
"  v_cvt_pk_bf16_f32 v46, v40, v41\n"
"  v_cvt_pk_bf16_f32 v47, v42, v43\n"
"  buffer_store_dwordx4 v[44:47], v0, s[40:43], s68 offen sc1\n"
"  s_mul_i32 s68, s51, 2176\n"
"  s_add_u32 s68, s68, 2154368\n"
"  v_mov_b32 v48, 1\n"
"  s_cmp_eq_u32 s63, 0\n"
"  s_cselect_b32 s68, s68, 0x7e000000\n"
"  s_waitcnt vmcnt(0)\n"
"  s_barrier\n"
"  buffer_atomic_add v48, v14, s[40:43], s68 offen sc1\n"
"  s_waitcnt vmcnt(0)\n"
"L_ce%=:\n"
            :: "s"(cp), "s"(h), "s"(w), "s"(slot)
            : CALL(v), CALL(a), C8(s, 2), C8(s, 3), C8(s, 4), C8(s, 5), C8(s, 6), C8(s, 7), C8(s, 8), C8(s, 9), "s100", "s101", "vcc", "scc", "memory");
        __builtin_amdgcn_endpgm();
    }
}

extern "C" void kernel_launch(void* const* d_in, const int* in_sizes, int n_in, void* d_out, int out_size, void* d_ws, size_t ws_size, hipStream_t stream) {
    char* ws = (char*)(((uintptr_t)d_ws + (2u << 20) - 1) & ~(uintptr_t)((2u << 20) - 1));
    int g_d1 = 120, g_d2 = 790, g_near = 0, g_mode = 750;
    (void)hipFuncSetAttribute((const void*)k_main, hipFuncAttributeMaxDynamicSharedMemorySize, 163840);
    Args a{(const u16*)d_in[0], (const u16*)d_in[2], (const u16*)d_in[3], (const u16*)d_in[4], (const u16*)d_in[5], (const u16*)d_in[8], (const u16*)d_in[9], (const u16*)d_in[10], (u16*)d_out, ws, g_d1, g_d2, g_near, g_mode, (const u16*)d_in[1]};
    k_main<<<256, 256, 163840, stream>>>(a);
}
```
